```python
import torch

def _softmax(x, axis=-1):
    return torch.softmax(x, dim=axis)

def _gelu(x):
    return torch.nn.functional.gelu(x, approximate='tanh')

def _silu(x):
    return torch.nn.functional.silu(x)

def _sigmoid(x):
    return torch.sigmoid(x)

def _layernorm(x, axis=-1, eps=1e-05):
    m = x.mean(dim=axis, keepdim=True)
    v = x.var(dim=axis, keepdim=True, unbiased=False)
    return (x - m) / torch.sqrt(v + eps)

def _scatter_add(tgt, idx, src, axis=0):
    return tgt.clone().index_add_(axis, idx, src)

def _segment_sum(src, seg, n):
    out = torch.zeros((n,) + src.shape[1:], dtype=src.dtype)
    return out.index_add_(0, seg, src)

def _pad(x, pw):
    flat = tuple((v for pair in reversed(pw) for v in pair))
    return torch.nn.functional.pad(x, flat)
kernel_name = 'concat_heads__memory_aug_recurrent_mem_tr__memory_aug_recurrent_mem_tr__prompt'

def setup_inputs():

    _w = lambda a: (lambda t: t.to(torch.bfloat16) if t.is_floating_point() else t)(torch.as_tensor(a))

    def _A_setup():
        B, S, Dm = (1, 2048, 640)
        H, Dh = (32, 192)
        M, seg = (8, 32)
        seg = min(seg, S)
        S = S // seg * seg
        return {'x': _w((lambda *s: torch.randn(*s, dtype=torch.bfloat16) * 0.05)(B, S, Dm)), 'mem0': _w((lambda *s: torch.randn(*s, dtype=torch.bfloat16) * 0.4)(B, M, Dm)), 'Wq': _w((lambda *s: torch.randn(*s, dtype=torch.bfloat16) * 6.4)(Dm, H, Dh) * Dm ** (-0.5)), 'Wk': _w((lambda *s: torch.randn(*s, dtype=torch.bfloat16) * 6.4)(Dm, H, Dh) * Dm ** (-0.5)), 'Wv': _w((lambda *s: torch.randn(*s, dtype=torch.bfloat16) * 0.4)(Dm, H, Dh) * Dm ** (-0.5)), 'Wo': _w((lambda *s: torch.randn(*s, dtype=torch.bfloat16) * 0.4)(H, Dh, Dm) * (H * Dh) ** (-0.5)), 'seg_len': seg}
    _A_in = _A_setup()

    def _B_setup():
        B, S, Dm = (1, 2048, 640)
        H, Dh = (32, 192)
        M, seg = (8, 32)
        seg = min(seg, S)
        S = S // seg * seg
        return {'x': _w((lambda *s: torch.randn(*s, dtype=torch.bfloat16) * 0.05)(B, S, Dm)), 'mem0': _w((lambda *s: torch.randn(*s, dtype=torch.bfloat16) * 0.4)(B, M, Dm)), 'Wq': _w((lambda *s: torch.randn(*s, dtype=torch.bfloat16) * 6.4)(Dm, H, Dh) * Dm ** (-0.5)), 'Wk': _w((lambda *s: torch.randn(*s, dtype=torch.bfloat16) * 6.4)(Dm, H, Dh) * Dm ** (-0.5)), 'Wv': _w((lambda *s: torch.randn(*s, dtype=torch.bfloat16) * 0.4)(Dm, H, Dh) * Dm ** (-0.5)), 'Wo': _w((lambda *s: torch.randn(*s, dtype=torch.bfloat16) * 0.4)(H, Dh, Dm) * (H * Dh) ** (-0.5)), 'seg_len': seg}
    _B_in = _B_setup()
    return {'A_x': _A_in['x'], 'A_mem0': _A_in['mem0'], 'A_Wq': _A_in['Wq'], 'A_Wk': _A_in['Wk'], 'A_Wv': _A_in['Wv'], 'A_Wo': _A_in['Wo'], 'A_seg_len': _A_in['seg_len'], 'B_x': _B_in['x'], 'B_mem0': _B_in['mem0'], 'B_Wq': _B_in['Wq'], 'B_Wk': _B_in['Wk'], 'B_Wv': _B_in['Wv'], 'B_Wo': _B_in['Wo'], 'B_seg_len': _B_in['seg_len']}

def reference(A_x, A_mem0, A_Wq, A_Wk, A_Wv, A_Wo, A_seg_len, B_x, B_mem0, B_Wq, B_Wk, B_Wv, B_Wo, B_seg_len):

    def _A_ref(x, mem0, Wq, Wk, Wv, Wo, seg_len):
        B, S, Dm = x.shape
        H, Dh = (Wq.shape[1], Wq.shape[2])
        M = mem0.shape[1]
        scale = Dh ** (-0.5)
        n_seg = S // seg_len
        L = M + seg_len
        idx = torch.arange(L)
        mask = (idx[:, None] < M) | (idx[:, None] >= idx[None, :])
        mem = mem0
        outs = []
        for s in range(n_seg):
            xs = x[:, s * seg_len:(s + 1) * seg_len, :].clone()
            h = torch.concatenate([mem, xs], axis=1)
            q = torch.einsum('btd,dhe->bhte', h, Wq)
            k = torch.einsum('btd,dhe->bhte', h, Wk)
            v = torch.einsum('btd,dhe->bhte', h, Wv)
            scores = torch.einsum('bhqe,bhke->bhqk', q, k) * scale
            scores = torch.where(mask[None, None], scores, -1000000000.0)
            p = _softmax(scores, axis=-1)
            o = torch.einsum('bhqk,bhke->bhqe', p, v)
            o = torch.einsum('bhte,hed->btd', o, Wo)
            mem = o[:, :M, :].clone()
            outs.append(o[:, M:, :])
        return torch.concatenate(outs, axis=1)

    def _B_ref(x, mem0, Wq, Wk, Wv, Wo, seg_len):
        B, S, Dm = x.shape
        H, Dh = (Wq.shape[1], Wq.shape[2])
        M = mem0.shape[1]
        scale = Dh ** (-0.5)
        n_seg = S // seg_len
        L = M + seg_len
        idx = torch.arange(L)
        mask = (idx[:, None] < M) | (idx[:, None] >= idx[None, :])
        mem = mem0
        outs = []
        for s in range(n_seg):
            xs = x[:, s * seg_len:(s + 1) * seg_len, :].clone()
            h = torch.concatenate([mem, xs], axis=1)
            q = torch.einsum('btd,dhe->bhte', h, Wq)
            k = torch.einsum('btd,dhe->bhte', h, Wk)
            v = torch.einsum('btd,dhe->bhte', h, Wv)
            scores = torch.einsum('bhqe,bhke->bhqk', q, k) * scale
            scores = torch.where(mask[None, None], scores, -1000000000.0)
            p = _softmax(scores, axis=-1)
            o = torch.einsum('bhqk,bhke->bhqe', p, v)
            o = torch.einsum('bhte,hed->btd', o, Wo)
            mem = o[:, :M, :].clone()
            outs.append(o[:, M:, :])
        return torch.concatenate(outs, axis=1)
    a = _A_ref(A_x, A_mem0, A_Wq, A_Wk, A_Wv, A_Wo, A_seg_len)
    b = _B_ref(B_x, B_mem0, B_Wq, B_Wk, B_Wv, B_Wo, B_seg_len)
    return torch.concatenate([a, b], axis=-1)
```

```cpp
#include <hip/hip_runtime.h>
#include <hip/hip_bf16.h>
#include <stdint.h>

typedef unsigned short u16;
static constexpr int S = 2048, DM = 640, H = 32, DH = 192, M = 8, SEG = 32, NSEG = 64, L = 40, HD = H * DH;

__device__ inline float bf2f(u16 v) { return __uint_as_float(((unsigned)v) << 16); }
__device__ inline u16 f2bf(float f) { __hip_bfloat16 b = __float2bfloat16(f); return *(u16*)&b; }
__device__ inline float rbf(float f) { return bf2f(f2bf(f)); }

__global__ void nv_gemm(const u16* A, int lda, const u16* B, int ldb, u16* C, int ldc, int rows, int N, int K) {
    int n = blockIdx.x * blockDim.x + threadIdx.x;
    int r0 = blockIdx.y * 8;
    if (n >= N) return;
    float acc[8] = {0, 0, 0, 0, 0, 0, 0, 0};
    for (int k = 0; k < K; ++k) {
        float b = bf2f(B[(size_t)k * ldb + n]);
#pragma unroll
        for (int i = 0; i < 8; ++i) acc[i] += bf2f(A[(size_t)(r0 + i) * lda + k]) * b;
    }
#pragma unroll
    for (int i = 0; i < 8; ++i) if (r0 + i < rows) C[(size_t)(r0 + i) * ldc + n] = f2bf(acc[i]);
}

__global__ void nv_attn(const u16* qm, const u16* km, const u16* vm, const u16* qx, const u16* kx, const u16* vx, u16* o) {
    int h = blockIdx.x;
    __shared__ float p[L][L];
    const float scale = rsqrtf((float)DH);
    for (int idx = threadIdx.x; idx < L * L; idx += blockDim.x) {
        int i = idx / L, j = idx % L;
        const u16* q = (i < M) ? qm + (size_t)i * HD + h * DH : qx + (size_t)(i - M) * HD + h * DH;
        const u16* k = (j < M) ? km + (size_t)j * HD + h * DH : kx + (size_t)(j - M) * HD + h * DH;
        float a = 0;
        for (int e = 0; e < DH; ++e) a += bf2f(q[e]) * bf2f(k[e]);
        a = rbf(rbf(a) * scale);
        bool ok = (i < M) || (i >= j);
        p[i][j] = ok ? a : -1e9f;
    }
    __syncthreads();
    if (threadIdx.x < L) {
        int i = threadIdx.x;
        float m = -INFINITY;
        for (int j = 0; j < L; ++j) m = fmaxf(m, p[i][j]);
        float s = 0;
        for (int j = 0; j < L; ++j) { p[i][j] = expf(p[i][j] - m); s += p[i][j]; }
        for (int j = 0; j < L; ++j) p[i][j] = rbf(p[i][j] / s);
    }
    __syncthreads();
    for (int idx = threadIdx.x; idx < L * DH; idx += blockDim.x) {
        int i = idx / DH, e = idx % DH;
        float a = 0;
        for (int j = 0; j < L; ++j) {
            const u16* v = (j < M) ? vm + (size_t)j * HD + h * DH : vx + (size_t)(j - M) * HD + h * DH;
            a += p[i][j] * bf2f(v[e]);
        }
        o[(size_t)i * HD + h * DH + e] = f2bf(a);
    }
}

extern "C" void kernel_launch(void* const* d_in, const int* in_sizes, int n_in,
                              void* d_out, int out_size, void* d_ws, size_t ws_size,
                              hipStream_t stream) {
    u16* ws = (u16*)d_ws;
    for (int b = 0; b < 2; ++b) {
        const u16* x = (const u16*)d_in[7 * b + 0];
        const u16* mem0 = (const u16*)d_in[7 * b + 1];
        const u16* Wq = (const u16*)d_in[7 * b + 2];
        const u16* Wk = (const u16*)d_in[7 * b + 3];
        const u16* Wv = (const u16*)d_in[7 * b + 4];
        const u16* Wo = (const u16*)d_in[7 * b + 5];
        u16* Qx = ws; u16* Kx = Qx + (size_t)S * HD; u16* Vx = Kx + (size_t)S * HD;
        u16* qm = Vx + (size_t)S * HD; u16* km = qm + M * HD; u16* vm = km + M * HD;
        u16* o = vm + M * HD; u16* mem = o + L * HD;
        u16* out = (u16*)d_out + b * DM;
        nv_gemm<<<dim3(HD / 256, S / 8), 256, 0, stream>>>(x, DM, Wq, HD, Qx, HD, S, HD, DM);
        nv_gemm<<<dim3(HD / 256, S / 8), 256, 0, stream>>>(x, DM, Wk, HD, Kx, HD, S, HD, DM);
        nv_gemm<<<dim3(HD / 256, S / 8), 256, 0, stream>>>(x, DM, Wv, HD, Vx, HD, S, HD, DM);
        for (int s = 0; s < NSEG; ++s) {
            const u16* mcur = (s == 0) ? mem0 : mem;
            nv_gemm<<<dim3(HD / 64, 1), 64, 0, stream>>>(mcur, DM, Wq, HD, qm, HD, M, HD, DM);
            nv_gemm<<<dim3(HD / 64, 1), 64, 0, stream>>>(mcur, DM, Wk, HD, km, HD, M, HD, DM);
            nv_gemm<<<dim3(HD / 64, 1), 64, 0, stream>>>(mcur, DM, Wv, HD, vm, HD, M, HD, DM);
            nv_attn<<<H, 256, 0, stream>>>(qm, km, vm, Qx + (size_t)s * SEG * HD, Kx + (size_t)s * SEG * HD, Vx + (size_t)s * SEG * HD, o);
            nv_gemm<<<dim3(DM / 64, 1), 64, 0, stream>>>(o, HD, Wo, DM, mem, DM, M, DM, HD);
            nv_gemm<<<dim3(DM / 64, 4), 64, 0, stream>>>(o + M * HD, HD, Wo, DM, out + (size_t)s * SEG * 2 * DM, 2 * DM, SEG, DM, HD);
        }
    }
}
```

```cpp
#include <hip/hip_runtime.h>
#include <stdint.h>

typedef unsigned u32;
typedef unsigned short u16;
typedef u32 v2u __attribute__((ext_vector_type(2)));
typedef u32 v4u __attribute__((ext_vector_type(4)));
typedef u32 v8u __attribute__((ext_vector_type(8)));
typedef float f4 __attribute__((ext_vector_type(4)));
typedef float f2 __attribute__((ext_vector_type(2)));
typedef short s4 __attribute__((ext_vector_type(4)));
typedef __bf16 b2 __attribute__((ext_vector_type(2)));
typedef __bf16 b8 __attribute__((ext_vector_type(8)));
typedef __bf16 b16 __attribute__((ext_vector_type(16)));
#define AS3 __attribute__((address_space(3)))
#define DI __device__ __forceinline__

constexpr int NSEG = 64;
constexpr u32 SENT = 0xFFFFFFFFu;
constexpr u32 OFF_FU = 0, OFF_FP = 8192, OFF_MP = 16384, MPB = 30720;
constexpr u32 OFF_Z = 2097152, ZB = 327680;
constexpr u32 OFF_U = OFF_Z + 22 * 1048576, UB = 1310720;
constexpr u32 OFF_PX = OFF_U + NSEG * UB, PXB = 81920;
constexpr u32 WS_END = OFF_PX + NSEG * PXB;
constexpr u32 WS_SPAN = ((WS_END + 8191) / 8192) * 16384;
constexpr u32 INIT_BYTES = ((OFF_MP + 3 * MPB + 8191) / 8192) * 16384;
constexpr u32 L_IMG = 98304, L_X = 122880, L_MEM = L_X + 24576, L_F = L_X + 34816, L_RED = L_X + 24576;
constexpr u32 L_VS = L_IMG + 12288, L_NRED = L_IMG + 16384;
constexpr u32 SPIN_MAX = 1u << 20;

struct Args { const u16* x[2]; const u16* mem0[2]; const u16* w[2][4]; u16* out; unsigned char* ws; };

__shared__ __attribute__((aligned(1024))) unsigned char lds[163840];

DI u32 amap(u32 off, u32 par) { u32 a = ((off >> 13) << 14) | (off & 8191u); return a | (((par ^ (a >> 21)) & 1u) << 13); }
DI u32 zoff(int s) { const u32 q = (u32)s / 3u; return OFF_Z + q * 1048576u + ((u32)s - 3u * q) * ZB; }
DI constexpr int wo_col(int i) { return i == 8 ? 0 : (i == 9 ? 4 : (i < 3 ? i + 1 : i + 2)); }
DI v4u lds128(u32 a) { return *(AS3 v4u*)((AS3 unsigned char*)lds + a); }
DI f2 lds64f(u32 a) { return *(AS3 f2*)((AS3 unsigned char*)lds + a); }
DI void st128(u32 a, v4u v) { *(AS3 v4u*)((AS3 unsigned char*)lds + a) = v; }
DI void st64(u32 a, v2u v) { *(AS3 v2u*)((AS3 unsigned char*)lds + a) = v; }
DI v2u ldtr(u32 a) { return __builtin_bit_cast(v2u, __builtin_amdgcn_ds_read_tr16_b64_v4i16((AS3 s4*)((AS3 unsigned char*)lds + a))); }
struct TB { u32 a, b; };
DI u32 opq(u32 v);
DI TB mktb(u32 a) { TB t = {opq(a), opq(a ^ 128u)}; return t; }
DI v4u trfrag(TB t, u32 off) { v2u lo = ldtr(t.a + off), hi = ldtr(t.b + off); v4u r = {lo.x, lo.y, hi.x, hi.y}; return r; }
DI int lane_id() { int l; asm volatile("v_mbcnt_lo_u32_b32 %0, -1, 0\n\tv_mbcnt_hi_u32_b32 %0, -1, %0" : "=v"(l)); return l; }
DI u32 opq(u32 v) { asm volatile("" : "+v"(v)); return v; }
DI v4u sentv() { const u32 v = opq(SENT); v4u r = {v, v, v, v}; return r; }
DI u32 rowp(int l) { return ((((l & 15) >> 2) ^ ((l >> 2) & 4)) * 32 + (l & 3) * 8); }
DI v8u cat8(v4u a, v4u b) { return __builtin_shufflevector(a, b, 0, 1, 2, 3, 4, 5, 6, 7); }
DI v4u lo4(v8u a) { return __builtin_shufflevector(a, a, 0, 1, 2, 3); }
DI v4u hi4(v8u a) { return __builtin_shufflevector(a, a, 4, 5, 6, 7); }
DI f4 mfma(v4u a, v4u b, f4 c) { return __builtin_amdgcn_mfma_f32_16x16x32_bf16(__builtin_bit_cast(b8, a), __builtin_bit_cast(b8, b), c, 0, 0, 0); }
DI f4 smf(v4u a, v8u b, f4 c, u32 idx) { return __builtin_amdgcn_smfmac_f32_16x16x64_bf16(__builtin_bit_cast(b8, a), __builtin_bit_cast(b16, b), c, (int)idx, 0, 0); }
DI u32 pk(float a, float b) { b2 r = {(__bf16)a, (__bf16)b}; return __builtin_bit_cast(u32, r); }
DI u32 pkc(float a, float b) { u32 r = pk(a, b); return r < 0xFFFFFFFEu ? r : 0xFFFFFFFEu; }
DI float rbf(float a) { return __uint_as_float(pk(a, a) & 0xFFFF0000u); }
DI void bar() { asm volatile("s_waitcnt lgkmcnt(0)\n\ts_barrier" ::: "memory"); }
DI void barv() { asm volatile("s_waitcnt vmcnt(0) lgkmcnt(0)\n\ts_barrier" ::: "memory"); }
DI void dma(const void* sbase, u32 voff, u32 ldst) {
    asm volatile("s_mov_b32 m0, %2\n\ts_nop 0\n\tglobal_load_lds_dwordx4 %0, %1" :: "v"(voff), "s"(sbase), "s"(ldst) : "memory");
}
DI float fold32(float a, float b) { auto s = __builtin_amdgcn_permlane32_swap(__float_as_uint(a), __float_as_uint(b), false, false); return __uint_as_float(s[0]) + __uint_as_float(s[1]); }
DI float xmax(float x) {
    auto s = __builtin_amdgcn_permlane16_swap(__float_as_uint(x), __float_as_uint(x), false, false); x = fmaxf(__uint_as_float(s[0]), __uint_as_float(s[1]));
    auto t = __builtin_amdgcn_permlane32_swap(__float_as_uint(x), __float_as_uint(x), false, false); return fmaxf(__uint_as_float(t[0]), __uint_as_float(t[1]));
}
DI float xsum(float x) {
    auto s = __builtin_amdgcn_permlane16_swap(__float_as_uint(x), __float_as_uint(x), false, false); x = __uint_as_float(s[0]) + __uint_as_float(s[1]);
    auto t = __builtin_amdgcn_permlane32_swap(__float_as_uint(x), __float_as_uint(x), false, false); return __uint_as_float(t[0]) + __uint_as_float(t[1]);
}
DI float ror8(float x) { return __uint_as_float(__builtin_amdgcn_update_dpp(0, __float_as_uint(x), 0x128, 0xF, 0xF, true)); }
DI u32 umax(u32 a, u32 b) { return a > b ? a : b; }
DI u32 max4(v4u v) { return umax(umax(v.x, v.y), umax(v.z, v.w)); }

struct Ctx {
    __amdgpu_buffer_rsrc_t r; u32 par; int h, p, wave; u32 spin;
    const u16* x; const u16* mem0; u16* out;
};
DI v4u gld(const Ctx& c, u32 off, u32 voff) { return __builtin_amdgcn_raw_buffer_load_b128(c.r, voff, amap(off, c.par), 16); }
DI void gst(const Ctx& c, u32 off, u32 voff, v4u v) { __builtin_amdgcn_raw_buffer_store_b128(v, c.r, voff, amap(off, c.par), 16); }
DI void gst64(const Ctx& c, u32 off, u32 voff, v2u v) { __builtin_amdgcn_raw_buffer_store_b64(v, c.r, voff, amap(off, c.par), 16); }
DI void gst32(const Ctx& c, u32 off, u32 voff, u32 v) { __builtin_amdgcn_raw_buffer_store_b32(v, c.r, voff, amap(off, c.par), 16); }
DI void gstv(const Ctx& c, u32 off, v4u v) { __builtin_amdgcn_raw_buffer_store_b128(v, c.r, amap(off, c.par), 0, 16); }

#define TS(c, k)
#define TSF(c, s)
#define SB() __builtin_amdgcn_sched_barrier(0)

template <int N> DI void poll(Ctx& c, const u32 (&off)[N], const u32 (&voff)[N], v4u (&v)[N]) {
    for (;;) {
        asm volatile("" ::: "memory");
        u32 m = 0;
#pragma unroll
        for (int k = 0; k < N; ++k) v[k] = gld(c, off[k], voff[k]);
#pragma unroll
        for (int k = 0; k < N; ++k) m = umax(m, max4(v[k]));
        if (__builtin_amdgcn_ballot_w64(m == SENT) == 0) return;
        if (++c.spin > SPIN_MAX) return;
    }
}

DI void load_x(const Ctx& c, int s, int f0, int n) {
    const int l = lane_id();
    const u32 voff = (l & 15) * 1280 + (l >> 4) * 16;
    const unsigned char* b = (const unsigned char*)c.x + (size_t)s * 40960;
    for (int i = 0; i < n; ++i) { int f = f0 + i; dma(b + (f & 1) * 20480 + (f >> 1) * 64, voff, L_X + f * 1024); }
}

DI void stage_mem(Ctx& c, int s) {
    if (c.wave < 5) {
        const int l = lane_id();
        v4u v[2];
        if (s == 0) {
            const __amdgpu_buffer_rsrc_t mr = __builtin_amdgcn_make_buffer_rsrc((void*)c.mem0, 0, 10240, 0x00020000);
            const u32 m = (l & 7) * 1280 + (l >> 4) * 32 + (l & 8) / 2;
#pragma unroll
            for (int k = 0; k < 2; ++k)
#pragma unroll
                for (int j = 0; j < 4; ++j) v[k][j] = __builtin_amdgcn_raw_buffer_load_b32(mr, m + k * 128 + j * 8, c.wave * 256, 0);
        } else {
            const u32 o = OFF_MP + s * MPB + c.wave * 2048;
            const u32 off[2] = {o, o + 1024}; const u32 voff[2] = {(u32)l * 16, (u32)l * 16};
            poll<2>(c, off, voff, v);
        }
        st128(L_MEM + c.wave * 2048 + l * 16, v[0]);
        st128(L_MEM + c.wave * 2048 + 1024 + l * 16, v[1]);
    }
    barv();
}

DI void node_duty(Ctx& c, int t, int n) {
    const int l = lane_id();
    const u32 l16 = l * 16;
    { const u32 off[1] = {OFF_FU + t * 128 + c.wave * 16}; const u32 voff[1] = {0}; v4u f[1]; poll<1>(c, off, voff, f); }
    v4u u[2][4];
#pragma unroll
    for (int j = 0; j < 2; ++j)
#pragma unroll
        for (int k = 0; k < 4; ++k) u[j][k] = gld(c, OFF_U + t * UB + ((2 * n + j) * 32 + 4 * c.wave + k) * 1024, l16);
    const u32 po = OFF_MP + t * MPB + 10240, zo = zoff(t) + (2 * n * 8 + c.wave) * 1024;
    const u32 off[5] = {po + c.wave * 2048, po + c.wave * 2048 + 1024, po + 16384 + c.wave * 512, zo, zo + 8192};
    const u32 voff[5] = {l16, l16, (u32)((l >> 4) * 128 + (l & 7) * 16), l16, l16};
    v4u v[5];
    TS(c, 5);
    poll<5>(c, off, voff, v);
    TS(c, 6);
    f4 lo[2], hi[2];
#pragma unroll
    for (int j = 0; j < 2; ++j) {
        const f4 z = {0, 0, 0, 0};
        lo[j] = mfma(u[j][0], v[0], z); hi[j] = mfma(u[j][1], v[0], z);
        lo[j] = mfma(u[j][2], v[1], lo[j]); hi[j] = mfma(u[j][3], v[1], hi[j]);
        lo[j] = mfma(v[3 + j], v[2], lo[j]);
    }
    f4 r;
#pragma unroll
    for (int i = 0; i < 4; ++i) { float a = lo[0][i] + ror8(hi[0][i]), b = ror8(lo[1][i]) + hi[1][i]; r[i] = (l & 8) ? b : a; }
    st128(L_NRED + c.wave * 1024 + l16, __builtin_bit_cast(v4u, r));
    const int r16 = l >> 2, jj = l & 3;
    const u32 a = L_NRED + (jj * 16 + c.wave * 8 + (r16 & 7)) * 16 + (r16 >> 3) * 8;
    bar();
    if (c.wave < 2) {
        f2 s = lds64f(a);
#pragma unroll
        for (int w = 1; w < 8; ++w) s += lds64f(a + w * 1024);
        gst32(c, OFF_MP + (t + 1) * MPB + (2 * n + c.wave) * 256, l * 4, pkc(s.x, s.y));
    }
    TS(c, 7);
}

DI void out_duty(Ctx& c, int t, int j) {
    const int l = lane_id();
    const u32 l16 = l * 16;
    { const u32 off[2] = {OFF_FU + t * 128 + c.wave * 16, OFF_FP + t * 128 + c.wave * 16}; const u32 voff[2] = {0, 0}; v4u f[2]; poll<2>(c, off, voff, f); }
    f4 a[2] = {{0, 0, 0, 0}, {0, 0, 0, 0}};
    {
        v4u u[4], px[4][2];
#pragma unroll
        for (int k = 0; k < 4; ++k) {
            u[k] = gld(c, OFF_U + t * UB + (j * 32 + 4 * c.wave + k) * 1024, l16);
            px[k][0] = gld(c, OFF_PX + t * PXB + ((4 * c.wave + k) * 2) * 1024, l16);
            px[k][1] = gld(c, OFF_PX + t * PXB + ((4 * c.wave + k) * 2 + 1) * 1024, l16);
        }
#pragma unroll
        for (int k = 0; k < 4; ++k) { a[0] = mfma(u[k], px[k][0], a[0]); a[1] = mfma(u[k], px[k][1], a[1]); }
    }
    {
        const u32 po = OFF_PX + t * PXB + 65536 + c.wave * 2048;
        const u32 off[3] = {zoff(t) + (j * 8 + c.wave) * 1024, po, po + 1024}; const u32 voff[3] = {l16, l16, l16};
        v4u z[3];
        poll<3>(c, off, voff, z);
        a[0] = mfma(z[0], z[1], a[0]); a[1] = mfma(z[0], z[2], a[1]);
    }
    st128(L_RED + c.wave * 2048 + l16, __builtin_bit_cast(v4u, a[0]));
    st128(L_RED + c.wave * 2048 + 1024 + l16, __builtin_bit_cast(v4u, a[1]));
    bar();
    if (c.wave < 2) {
        f4 s = __builtin_bit_cast(f4, lds128(L_RED + c.wave * 1024 + l16));
#pragma unroll
        for (int w = 1; w < 8; ++w) s += __builtin_bit_cast(f4, lds128(L_RED + (w * 2 + c.wave) * 1024 + l16));
        v2u o = {pk(s[0], s[1]), pk(s[2], s[3])};
        const __amdgpu_buffer_rsrc_t orr = __builtin_amdgcn_make_buffer_rsrc((void*)c.out, 0, 2048 * 2560 - 1280, 0x00020000);
        __builtin_amdgcn_raw_buffer_store_b64(o, orr, (l & 15) * 2560 + (l >> 4) * 8, (t * 32 + c.wave * 16) * 2560 + j * 32, 0);
    }
    bar();
}

template <bool WO> DI TB wbase(const Ctx& c, int l) {
    return mktb(WO ? 12288 + (l >> 4) * 1024 + (c.wave & 3) * 256 + rowp(l) : (c.wave >> 2) * 12288 + (l >> 4) * 3072 + (c.wave & 3) * 768 + rowp(l));
}
template <bool WO> DI void issue_chunk(const unsigned char* wb, u32 voff, int wave, int ch) {
    const u32 slot = ((ch + 2) % 6) * 24576;
#pragma unroll
    for (int u = 0; u < 3; ++u) {
        if (WO) dma(wb + (size_t)(3 * (ch & 1) + u) * 40960 + wo_col(ch >> 1) * 128, voff, slot + 12288 + (u * 4 + (wave & 3)) * 1024);
        else dma(wb + (size_t)ch * 393216 + u * 128, voff, slot + (wave >> 2) * 12288 + (wave & 3) * 3072 + u * 1024);
    }
}
template <bool WO> DI void load_weights(const Ctx& c, const u16* w, v8u (&W)[24]) {
    const int l = lane_id();
    const int kb = c.wave & 3, rr = ((l >> 1) & 7) ^ (4 * (kb & 1));
    const u32 voff = (8 * kb + rr) * (WO ? 1280 : 12288) + (l >> 4) * 32 + (l & 1) * 16;
    const unsigned char* wb = (const unsigned char*)w + (WO ? (size_t)c.h * 245760 : (size_t)c.h * 384);
    const TB wl = wbase<WO>(c, l);
#pragma unroll
    for (int i = 0; i < 5; ++i) issue_chunk<WO>(wb, voff, c.wave, i);
#pragma unroll
    for (int i = 0; i < 16; ++i) {
        asm volatile("s_waitcnt vmcnt(12) lgkmcnt(0)\n\ts_barrier" ::: "memory");
        if (i < 15) issue_chunk<WO>(wb, voff, c.wave, i + 5);
        const u32 slot = ((i + 2) % 6) * 24576;
#pragma unroll
        for (int u = 0; u < 3; ++u) {
            v4u f = trfrag(wl, slot + u * (WO ? 4096 : 256));
            const int ks = 3 * (i & 1) + u;
            const int pi = WO ? (i >> 1) * 3 + (ks >> 1) : u * 8 + (i >> 1), hf = WO ? (ks & 1) : (i & 1);
            W[pi] = hf ? cat8(lo4(W[pi]), f) : cat8(f, f);
        }
    }
    barv();
}

DI void crit_pre(TB wl, v8u (&T)[3]) {
#pragma unroll
    for (int u = 0; u < 3; ++u) T[u] = cat8(trfrag(wl, u * 256), trfrag(wl, 24576 + u * 256));
}
DI void crit_gemm1(const v8u (&W)[24], v8u (&T)[3], TB wl, int l, v2u& r01, v2u& r2) {
    const u32 idx = 0x4444u + (l & 8) * 0x1555u + ((l & 8) >> 2);
    f4 acc[3] = {{0, 0, 0, 0}, {0, 0, 0, 0}, {0, 0, 0, 0}};
    v4u a[11];
    const u32 ab = opq(L_MEM + l * 16);
    a[0] = lds128(ab + 8192);
#pragma unroll
    for (int i = 1; i < 4; ++i) a[i] = lds128(ab + (i - 1) * 1024);
    SB();
#pragma unroll
    for (int i = 0; i < 10; ++i) {
#pragma unroll
        for (int u = 0; u < 3; ++u) acc[u] = smf(a[i], (i == 0 || i == 9) ? T[u] : W[u * 8 + i - 1], acc[u], idx);
        SB();
        if (i + 4 < 10) a[i + 4] = lds128(ab + (i + 4 == 9 ? 9 : i + 3) * 1024);
        if (i >= 1 && i < 4) T[i - 1] = cat8(trfrag(wl, 49152 + (i - 1) * 256), trfrag(wl, 73728 + (i - 1) * 256));
        SB();
    }
    float s[4], t[4];
#pragma unroll
    for (int i = 0; i < 4; ++i) { s[i] = fold32(acc[0][i], acc[1][i]); t[i] = fold32(acc[2][i], acc[2][i]); }
    r01.x = pk(s[0], s[1]); r01.y = pk(s[2], s[3]); r2.x = pk(t[0], t[1]); r2.y = pk(t[2], t[3]);
}

template <class F> DI void bulk_gemm1(const Ctx& c, const v8u (&W)[24], F mid) {
    const int l = lane_id();
    const TB wl = wbase<false>(c, l);
    const u32 ab = opq(L_X + l * 16);
    f4 acc[2][3];
#pragma unroll
    for (int m = 0; m < 2; ++m)
#pragma unroll
        for (int u = 0; u < 3; ++u) acc[m][u] = f4{0, 0, 0, 0};
    v4u a[21][2], b[21][3];
    a[0][0] = lds128(ab); a[0][1] = lds128(ab + 1024);
#pragma unroll
    for (int ks = 0; ks < 20; ++ks) {
        if (ks == 11) mid(0);
        if (ks == 16) mid(1);
        if (ks < 19) { a[ks + 1][0] = lds128(ab + (ks + 1) * 2048); a[ks + 1][1] = lds128(ab + (ks + 1) * 2048 + 1024); }
        if (ks == 15) {
#pragma unroll
            for (int u = 0; u < 3; ++u) b[16][u] = trfrag(wl, u * 256);
        }
        SB();
#pragma unroll
        for (int u = 0; u < 3; ++u) {
            const v4u bb = ks < 16 ? ((ks & 1) ? hi4(W[u * 8 + (ks >> 1)]) : lo4(W[u * 8 + (ks >> 1)])) : b[ks][u];
            acc[0][u] = mfma(a[ks][0], bb, acc[0][u]); acc[1][u] = mfma(a[ks][1], bb, acc[1][u]);
            if (ks >= 16 && ks < 19) { SB(); b[ks + 1][u] = trfrag(wl, (ks - 15) * 24576 + u * 256); SB(); }
        }
        SB();
    }
    const int cc = l & 15;
    const u32 ia = L_IMG + (c.wave >> 2) * 12288 + ((c.wave & 3) * 48 + (cc ^ (4 * (cc >> 3)))) * 32 + (l >> 4) * 8;
#pragma unroll
    for (int m = 0; m < 2; ++m)
#pragma unroll
        for (int u = 0; u < 3; ++u) { v2u v = {pk(acc[m][u][0], acc[m][u][1]), pk(acc[m][u][2], acc[m][u][3])}; st64(ia + m * 6144 + u * 512, v); }
}

DI void duty(Ctx& c, int s, bool vm) {
    if (vm && c.h < 20) { if (s + 1 < NSEG - 1) node_duty(c, s + 1, c.h); }
    else if (s >= 0 && (!vm || c.h < 28)) out_duty(c, s, vm ? c.h + 12 : c.h);
}

DI void qk_main(Ctx& c, const u16* wq, const u16* wk) {
    v8u W[24];
    load_x(c, c.p, 24 + 2 * c.wave, 2);
    {
        const v4u sv = sentv();
        for (int i = c.wave * 64 + lane_id(); i < 31 * 40; i += 512) {
            const int st = c.p + 2 + 2 * (i / 40), e = i % 40;
            const u32 o = e < 32 ? (c.h >> 1) * 1024 + ((e >> 3) * 16 + (c.h & 1) * 8 + (e & 7)) * 16 : 16384 + c.h * 128 + (e & 7) * 16;
            gstv(c, OFF_MP + st * MPB + 10240 + o, sv);
        }
        asm volatile("s_waitcnt vmcnt(0)" ::: "memory");
    }
    load_weights<false>(c, c.wave < 4 ? wq : wk, W);
    load_x(c, c.p, 3 * c.wave, 3);
    barv();
    for (int s = c.p - 2; s < NSEG; s += 2) {
        asm volatile("" : "+s"(c.wave), "+s"(c.h), "+s"(c.par));
        if (s >= 0) {
            const int l = lane_id(), g = l >> 4, cc = l & 15;
            const TB wl = wbase<false>(c, l);
            v8u T[3];
            crit_pre(wl, T);
            TS(c, 0);
            stage_mem(c, s);
            TS(c, 1);
            v2u r01, r2;
            crit_gemm1(W, T, wl, l, r01, r2);
            const u32 fa = L_F + ((c.wave & 3) * 48 + (cc ^ (4 * (cc >> 3)))) * 32 + (c.wave >> 2) * 16 + (g & 1) * 8;
            st64(fa + (g >> 1) * 512, r01);
            if (g < 2) st64(fa + 1024, r2);
            const TB il = mktb(L_IMG + g * 256 + rowp(l));
            f4 t[3] = {{0, 0, 0, 0}, {0, 0, 0, 0}, {0, 0, 0, 0}};
            if (c.wave == 0) {
                __builtin_amdgcn_s_setprio(3);
                v4u k[6][2], f[6];
#pragma unroll
                for (int ks = 0; ks < 3; ++ks) { k[ks][0] = trfrag(il, 12288 + ks * 1024); k[ks][1] = trfrag(il, 18432 + ks * 1024); }
                bar();
                TS(c, 2);
#pragma unroll
                for (int ks = 0; ks < 4; ++ks) f[ks] = trfrag(il, L_F - L_IMG + ks * 1024);
                SB();
#pragma unroll
                for (int ks = 0; ks < 6; ++ks) {
                    t[1] = mfma(k[ks][0], f[ks], t[1]); t[2] = mfma(k[ks][1], f[ks], t[2]); t[0] = mfma(f[ks], f[ks], t[0]);
                    SB();
                    if (ks < 3) { k[ks + 3][0] = trfrag(il, 12288 + (ks + 3) * 1024); k[ks + 3][1] = trfrag(il, 18432 + (ks + 3) * 1024); }
                    if (ks < 2) f[ks + 4] = trfrag(il, L_F - L_IMG + (ks + 4) * 1024);
                    SB();
                }
            } else {
                bar();
                TS(c, 2);
                if (c.wave < 3) {
                    const TB bl = {il.a + (c.wave - 1) * 6144, il.b + (c.wave - 1) * 6144};
                    v4u k[7][4];
                    k[0][0] = trfrag(bl, 0); k[0][1] = trfrag(il, L_F - L_IMG); k[0][2] = trfrag(il, 12288); k[0][3] = trfrag(il, 18432);
#pragma unroll
                    for (int ks = 0; ks < 6; ++ks) {
                        if (ks < 5) { const u32 o = (ks + 1) * 1024; k[ks + 1][0] = trfrag(bl, o); k[ks + 1][1] = trfrag(il, L_F - L_IMG + o); k[ks + 1][2] = trfrag(il, 12288 + o); k[ks + 1][3] = trfrag(il, 18432 + o); }
                        SB();
                        t[0] = mfma(k[ks][1], k[ks][0], t[0]); t[1] = mfma(k[ks][2], k[ks][0], t[1]); t[2] = mfma(k[ks][3], k[ks][0], t[2]);
                        SB();
                    }
                } else if (s + 2 < NSEG) load_x(c, s + 2, 24 + 2 * (c.wave - 3), 2);
            }
            if (c.wave < 3) {
                const float scale = 0.07216878364870322f, l2e = 1.4426950408889634f, ninf = __uint_as_float(opq(0xFF800000u));
                const int lim = c.wave == 0 ? 64 : (c.wave - 1) * 16 + cc - 4 * g;
                float mx = ninf;
#pragma unroll
                for (int k = 0; k < 3; ++k)
#pragma unroll
                    for (int i = 0; i < 4; ++i) {
                        float v = rbf(rbf(t[k][i]) * scale);
                        const bool vis = k == 0 ? g >= 2 : (16 * (k - 1) + i <= lim);
                        v = vis ? v : ninf;
                        t[k][i] = v; mx = fmaxf(mx, v);
                    }
                mx = xmax(mx);
                float sum = 0;
#pragma unroll
                for (int k = 0; k < 3; ++k)
#pragma unroll
                    for (int i = 0; i < 4; ++i) { t[k][i] = __builtin_amdgcn_exp2f((t[k][i] - mx) * l2e); sum += t[k][i]; }
                sum = xsum(sum);
                const float inv = __builtin_amdgcn_rcpf(sum);
                const v4u px = {pkc(t[1][0] * inv, t[1][1] * inv), pkc(t[1][2] * inv, t[1][3] * inv), pkc(t[2][0] * inv, t[2][1] * inv), pkc(t[2][2] * inv, t[2][3] * inv)};
                const v2u pm = {pkc(t[0][0] * inv, t[0][1] * inv), pkc(t[0][2] * inv, t[0][3] * inv)};
                if (c.wave == 0) {
                    const u32 po = OFF_MP + s * MPB + 10240;
                    if (cc < 8) {
                        gst(c, po + (c.h >> 1) * 1024, (g * 16 + (c.h & 1) * 8 + cc) * 16, px);
                        if (g >= 2) gst64(c, po + 16384 + c.h * 128, cc * 16 + (g & 1) * 8, pm);
                    }
                    __builtin_amdgcn_s_setprio(0);
                } else {
                    const u32 po = OFF_PX + s * PXB;
                    gst(c, po + (c.h * 2 + c.wave - 1) * 1024, l * 16, px);
                    if (g >= 2) gst64(c, po + 65536 + ((c.h >> 2) * 2 + c.wave - 1) * 1024 + (c.h & 3) * 256, cc * 16 + (g & 1) * 8, pm);
                }
            }
            TS(c, 3);
        }
        auto mid = [&](int k) {
            barv();
            if (k == 0 && s >= 0) {
                if (c.wave == 7) gst32(c, OFF_FP + s * 128 + c.h * 4, 0, opq(1u));
                else if (c.wave < 6 && s + 2 < NSEG) load_x(c, s + 2, 34 + c.wave, 1);
            }
        };
        if (s + 2 < NSEG) {
            bulk_gemm1(c, W, mid);
            bar();
            if (s + 4 < NSEG) load_x(c, s + 4, 3 * c.wave, 3);
        } else mid(0);
        TS(c, 4);
        duty(c, s, false);
        TSF(c, s);
    }
}

DI void clear_z(const Ctx& c, int s) {
    const int l = lane_id();
    const v4u sv = sentv();
    for (int i = c.wave; i < 10; i += 4)
        gst(c, zoff(s) + i * 32768 + c.h * 256, (l >> 4) * 16384 + (l & 15) * 16, sv);
}
DI void flag_u(const Ctx& c, int s) { barv(); if (c.wave == 7) gst32(c, OFF_FU + s * 128 + c.h * 4, 0, opq(1u)); }

DI void v_main(Ctx& c, const u16* wv) {
    v8u W[24];
    load_x(c, c.p, 24 + 4 * c.wave, 4);
    if (c.h < 20) {
        const v4u sv = sentv();
        for (int i = c.wave * 64 + lane_id(); i < 31 * 32; i += 256) {
            const int st = 4 - c.p + 2 * (i >> 5);
            if (st < NSEG) gstv(c, OFF_MP + st * MPB + c.h * 512 + (i & 31) * 16, sv);
        }
    }
    asm volatile("s_waitcnt vmcnt(0)" ::: "memory");
    load_weights<false>(c, wv, W);
    load_x(c, c.p, 6 * c.wave, 6);
    barv();
    for (int s = c.p - 2; s < NSEG; s += 2) {
        asm volatile("" : "+s"(c.wave), "+s"(c.h), "+s"(c.par));
        if (s >= 0) {
            const int l = lane_id(), g = l >> 4, cc = l & 15;
            const TB wl = wbase<false>(c, l);
            v8u T[3];
            crit_pre(wl, T);
            TS(c, 0);
            stage_mem(c, s);
            TS(c, 1);
            v2u r01, r2;
            crit_gemm1(W, T, wl, l, r01, r2);
            const int rho = ((cc >> 2) << 1) | (cc & 1);
            const u32 a = L_VS + ((cc >> 1) & 1) * 16 + (g & 1) * 8;
            const int t0 = c.wave * 3 + (g >> 1), t2 = c.wave * 3 + 2;
            st64(a + (t0 * 8 + (rho ^ (4 * (t0 & 1)))) * 32, r01);
            if (g < 2) st64(a + (t2 * 8 + (rho ^ (4 * (t2 & 1)))) * 32, r2);
            bar();
            TS(c, 2);
        }
        if (s + 2 < NSEG) {
            if (s >= 0) load_x(c, s + 2, 24 + 4 * c.wave, 4);
            bulk_gemm1(c, W, [&](int k) { if (k == 0) barv(); });
            bar();
            if (s + 4 < NSEG) load_x(c, s + 4, 6 * c.wave, 6);
            clear_z(c, s + 2);
            flag_u(c, s + 2);
        }
        TS(c, 4);
        if (s >= -1) duty(c, s, true);
        TSF(c, s);
    }
}

DI u32 wo_off(int i, int ks) { return ((i - 8) * 2 + ks / 3) * 24576 + (ks % 3) * 4096; }
DI void bulk_gemm2(const Ctx& c, const v8u (&W)[24], int s) {
    const int l = lane_id();
    const TB wl = wbase<true>(c, l), il = mktb(L_IMG + (l >> 4) * 256 + rowp(l));
    v4u a[19][2], b[19];
    a[0][0] = trfrag(il, 0); a[0][1] = trfrag(il, 6144);
#pragma unroll
    for (int ig = 0; ig < 3; ++ig) {
        const int nt = ig == 0 ? 4 : 3;
        f4 acc[2][4];
#pragma unroll
        for (int j = 0; j < 4; ++j) { acc[0][j] = f4{0, 0, 0, 0}; acc[1][j] = f4{0, 0, 0, 0}; }
#pragma unroll
        for (int ks = 0; ks < 6; ++ks) {
            const int st = ig * 6 + ks, n1 = (st + 1) % 6;
            if (st < 17) { a[st + 1][0] = trfrag(il, n1 * 1024); a[st + 1][1] = trfrag(il, 6144 + n1 * 1024); }
            if (st >= 5 && st < 17) b[st + 1] = trfrag(wl, wo_off(7 + (st + 1) / 6, n1));
            SB();
#pragma unroll
            for (int j = 0; j < nt; ++j) {
                const int i = ig == 0 ? j : (j < 2 ? 2 + 2 * ig + j : 7 + ig);
                const v4u bb = i < 8 ? ((ks & 1) ? hi4(W[i * 3 + (ks >> 1)]) : lo4(W[i * 3 + (ks >> 1)])) : b[st];
                acc[0][j] = mfma(a[st][0], bb, acc[0][j]); acc[1][j] = mfma(a[st][1], bb, acc[1][j]);
            }
            SB();
        }
#pragma unroll
        for (int j = 0; j < nt; ++j) {
            const int i = ig == 0 ? j : (j < 2 ? 2 + 2 * ig + j : 7 + ig);
            const v4u v = {pk(acc[0][j][0], acc[0][j][1]), pk(acc[0][j][2], acc[0][j][3]), pk(acc[1][j][0], acc[1][j][1]), pk(acc[1][j][2], acc[1][j][3])};
            gst(c, OFF_U + s * UB + ((wo_col(i) * 4 + (c.wave & 3)) * 32 + c.h) * 1024, l * 16, v);
        }
        SB();
    }
}

DI void o_main(Ctx& c, const u16* wo) {
    v8u W[24];
    load_weights<true>(c, wo, W);
    barv();
    for (int s = c.p - 2; s < NSEG; s += 2) {
        asm volatile("" : "+s"(c.wave), "+s"(c.h), "+s"(c.par));
        if (s >= 0) {
            const int l = lane_id(), g = l >> 4;
            const TB wl = wbase<true>(c, l), il = mktb(L_VS + g * 256 + rowp(l));
            const u32 idx = 0x4444u + (l & 8) * 0x1555u + ((l & 8) >> 2);
            const u32 za = ((g >> 1) + 2 * (g & 1)) * 65536 + (l & 15) * 16;
            stage_mem(c, s);
            v8u T[3];
#pragma unroll
            for (int f = 0; f < 3; ++f) T[f] = cat8(trfrag(wl, wo_off(8, 2 * f)), trfrag(wl, wo_off(8, 2 * f + 1)));
            __builtin_amdgcn_s_setprio(3);
            bar();
            TS(c, 2);
            v4u a[3];
#pragma unroll
            for (int f = 0; f < 3; ++f) a[f] = trfrag(il, f * 1024);
            SB();
            f4 acc[6][2];
            u32 xp[5][2];
#pragma unroll
            for (int pr = 0; pr < 6; ++pr) {
                if (pr < 5) {
                    acc[pr][0] = f4{0, 0, 0, 0}; acc[pr][1] = f4{0, 0, 0, 0};
#pragma unroll
                    for (int f = 0; f < 3; ++f)
#pragma unroll
                        for (int j = 0; j < 2; ++j) {
                            const int o = 2 * pr + j, i = o == 0 ? 8 : (o == 4 ? 9 : (o < 4 ? o - 1 : o - 2));
                            acc[pr][j] = smf(a[f], i < 8 ? W[i * 3 + f] : T[f], acc[pr][j], idx);
                        }
                }
                if (pr == 0) {
                    SB();
#pragma unroll
                    for (int f = 0; f < 3; ++f) T[f] = cat8(trfrag(wl, wo_off(9, 2 * f)), trfrag(wl, wo_off(9, 2 * f + 1)));
                } else {
                    const int k = pr - 1;
                    float x[4];
#pragma unroll
                    for (int i = 0; i < 4; ++i) x[i] = fold32(acc[k][0][i], acc[k][1][i]);
                    xp[k][0] = pkc(x[0], x[1]); xp[k][1] = pkc(x[2], x[3]);
                    if (k == 1 || k == 3 || k == 4) {
                        const int k0 = k == 4 ? 4 : k - 1;
                        auto s0 = __builtin_amdgcn_permlane16_swap(xp[k0][0], xp[k][0], false, false);
                        auto s1 = __builtin_amdgcn_permlane16_swap(xp[k0][1], xp[k][1], false, false);
                        const v4u v = {s0[0], s1[0], s0[1], s1[1]};
                        if (k < 4 || !(g & 1)) gst(c, zoff(s) + (k >> 1) * 131072 + (c.wave & 3) * 8192 + c.h * 256, za, v);
                    }
                }
                SB();
            }
            __builtin_amdgcn_s_setprio(0);
            TS(c, 3);
        }
        if (s + 2 < NSEG) {
            bar();
            bar();
            bulk_gemm2(c, W, s + 2);
            flag_u(c, s + 2);
        }
        TS(c, 4);
        if (s >= -1) duty(c, s, true);
        TSF(c, s);
    }
}

__global__ void __launch_bounds__(512, 2) memtr_kernel(Args a) {
    Ctx c;
    const int wg = blockIdx.x;
    c.wave = __builtin_amdgcn_readfirstlane(threadIdx.x >> 6);
    const int br = (wg & 7) >> 2;
    const int idx = (wg >> 3) * 4 + (wg & 3);
    c.h = idx & 31; c.p = idx >> 6;
    c.r = __builtin_amdgcn_make_buffer_rsrc(a.ws, 0, WS_SPAN, 0x00020000);
    c.par = br ^ (u32)(((size_t)a.ws >> 21) & 1);
    c.spin = 0;
    c.x = br ? a.x[1] : a.x[0]; c.mem0 = br ? a.mem0[1] : a.mem0[0]; c.out = a.out + br * 640;
    if (((idx >> 5) & 1) == 0) qk_main(c, br ? a.w[1][0] : a.w[0][0], br ? a.w[1][1] : a.w[0][1]);
    else if (c.wave < 4) v_main(c, br ? a.w[1][2] : a.w[0][2]);
    else o_main(c, br ? a.w[1][3] : a.w[0][3]);
}

extern "C" void kernel_launch(void* const* d_in, const int* in_sizes, int n_in, void* d_out, int out_size, void* d_ws, size_t ws_size, hipStream_t stream) {
    Args a;
    for (int b = 0; b < 2; ++b) {
        a.x[b] = (const u16*)d_in[7 * b]; a.mem0[b] = (const u16*)d_in[7 * b + 1];
        for (int k = 0; k < 4; ++k) a.w[b][k] = (const u16*)d_in[7 * b + 2 + k];
    }
    a.out = (u16*)d_out; a.ws = (unsigned char*)d_ws;
    (void)hipMemsetAsync(d_ws, 0xFF, INIT_BYTES, stream);
    memtr_kernel<<<256, 512, 0, stream>>>(a);
}
```

```cpp
#include <hip/hip_runtime.h>
#include <stdint.h>

typedef unsigned u32;
typedef unsigned short u16;
typedef u32 v2u __attribute__((ext_vector_type(2)));
typedef u32 v4u __attribute__((ext_vector_type(4)));
typedef u32 v8u __attribute__((ext_vector_type(8)));
typedef float f4 __attribute__((ext_vector_type(4)));
typedef float f2 __attribute__((ext_vector_type(2)));
typedef short s4 __attribute__((ext_vector_type(4)));
typedef __bf16 b2 __attribute__((ext_vector_type(2)));
typedef __bf16 b8 __attribute__((ext_vector_type(8)));
typedef __bf16 b16 __attribute__((ext_vector_type(16)));
#define AS3 __attribute__((address_space(3)))
#define DI __device__ __forceinline__

constexpr int NSEG = 64;
constexpr u32 SENT = 0xFFFFFFFFu;
constexpr u32 OFF_FU = 8192, OFF_FP = 40960, OFF_MP = 49152, MPB = 30720;
constexpr u32 OFF_Z = 2097152, ZB = 327680;
constexpr u32 OFF_U = 4194304, UB = 1310720;
constexpr u32 OFF_PX = OFF_U + 6 * UB, PXB = 81920;
constexpr int NPAGE = 12;
constexpr u32 WS_SPAN = NPAGE * 2097152;
constexpr u32 INIT_BYTES = ((OFF_MP + 5 * MPB + 8191) / 8192) * 16384;
constexpr u32 L_IMG = 98304, L_X = 122880, L_MEM = L_X + 24576, L_F = L_X + 34816, L_RED = L_X + 24576;
constexpr u32 L_VS = L_IMG + 12288, L_NRED = L_IMG + 16384;
constexpr u32 SPIN_MAX = 1u << 20;

struct Args { const u16* x[2]; const u16* mem0[2]; const u16* w[2][4]; u16* out; unsigned char* ws; };

__shared__ __attribute__((aligned(1024))) unsigned char lds[163840];

DI u32 amap(u32 off, u32 par) { u32 a = ((off >> 13) << 14) | (off & 8191u); return a | (((par >> (a >> 21)) & 1u) << 13); }
DI u32 zoff(int s) { const u32 q = (u32)s / 3u; return OFF_Z + (q & 1u) * 1048576u + ((u32)s - 3u * q) * ZB; }
DI u32 uoff(int s) { return OFF_U + ((u32)s % 6u) * UB; }
DI u32 pxoff(int s) { return OFF_PX + ((u32)s % 6u) * PXB; }
DI constexpr int wo_col(int i) { return i == 8 ? 0 : (i == 9 ? 4 : (i < 3 ? i + 1 : i + 2)); }
DI v4u lds128(u32 a) { return *(AS3 v4u*)((AS3 unsigned char*)lds + a); }
DI f2 lds64f(u32 a) { return *(AS3 f2*)((AS3 unsigned char*)lds + a); }
DI void st128(u32 a, v4u v) { *(AS3 v4u*)((AS3 unsigned char*)lds + a) = v; }
DI void st64(u32 a, v2u v) { *(AS3 v2u*)((AS3 unsigned char*)lds + a) = v; }
DI v2u ldtr(u32 a) { return __builtin_bit_cast(v2u, __builtin_amdgcn_ds_read_tr16_b64_v4i16((AS3 s4*)((AS3 unsigned char*)lds + a))); }
struct TB { u32 a, b; };
DI u32 opq(u32 v);
DI TB mktb(u32 a) { TB t = {opq(a), opq(a ^ 128u)}; return t; }
DI v4u trfrag(TB t, u32 off) { v2u lo = ldtr(t.a + off), hi = ldtr(t.b + off); v4u r = {lo.x, lo.y, hi.x, hi.y}; return r; }
DI int lane_id() { int l; asm volatile("v_mbcnt_lo_u32_b32 %0, -1, 0\n\tv_mbcnt_hi_u32_b32 %0, -1, %0" : "=v"(l)); return l; }
DI u32 opq(u32 v) { asm volatile("" : "+v"(v)); return v; }
DI v4u sentv() { const u32 v = opq(SENT); v4u r = {v, v, v, v}; return r; }
DI u32 rowp(int l) { return ((((l & 15) >> 2) ^ ((l >> 2) & 4)) * 32 + (l & 3) * 8); }
DI v8u cat8(v4u a, v4u b) { return __builtin_shufflevector(a, b, 0, 1, 2, 3, 4, 5, 6, 7); }
DI v4u lo4(v8u a) { return __builtin_shufflevector(a, a, 0, 1, 2, 3); }
DI v4u hi4(v8u a) { return __builtin_shufflevector(a, a, 4, 5, 6, 7); }
DI f4 mfma(v4u a, v4u b, f4 c) { return __builtin_amdgcn_mfma_f32_16x16x32_bf16(__builtin_bit_cast(b8, a), __builtin_bit_cast(b8, b), c, 0, 0, 0); }
DI f4 smf(v4u a, v8u b, f4 c, u32 idx) { return __builtin_amdgcn_smfmac_f32_16x16x64_bf16(__builtin_bit_cast(b8, a), __builtin_bit_cast(b16, b), c, (int)idx, 0, 0); }
DI u32 pk(float a, float b) { b2 r = {(__bf16)a, (__bf16)b}; return __builtin_bit_cast(u32, r); }
DI u32 pkc(float a, float b) { u32 r = pk(a, b); return r < 0xFFFFFFFEu ? r : 0xFFFFFFFEu; }
DI float rbf(float a) { return __uint_as_float(pk(a, a) & 0xFFFF0000u); }
DI void bar() { asm volatile("s_waitcnt lgkmcnt(0)\n\ts_barrier" ::: "memory"); }
DI void barv() { asm volatile("s_waitcnt vmcnt(0) lgkmcnt(0)\n\ts_barrier" ::: "memory"); }
DI void dma(const void* sbase, u32 voff, u32 ldst) {
    asm volatile("s_mov_b32 m0, %2\n\ts_nop 0\n\tglobal_load_lds_dwordx4 %0, %1" :: "v"(voff), "s"(sbase), "s"(ldst) : "memory");
}
DI float fold32(float a, float b) { auto s = __builtin_amdgcn_permlane32_swap(__float_as_uint(a), __float_as_uint(b), false, false); return __uint_as_float(s[0]) + __uint_as_float(s[1]); }
DI float xmax(float x) {
    auto s = __builtin_amdgcn_permlane16_swap(__float_as_uint(x), __float_as_uint(x), false, false); x = fmaxf(__uint_as_float(s[0]), __uint_as_float(s[1]));
    auto t = __builtin_amdgcn_permlane32_swap(__float_as_uint(x), __float_as_uint(x), false, false); return fmaxf(__uint_as_float(t[0]), __uint_as_float(t[1]));
}
DI float xsum(float x) {
    auto s = __builtin_amdgcn_permlane16_swap(__float_as_uint(x), __float_as_uint(x), false, false); x = __uint_as_float(s[0]) + __uint_as_float(s[1]);
    auto t = __builtin_amdgcn_permlane32_swap(__float_as_uint(x), __float_as_uint(x), false, false); return __uint_as_float(t[0]) + __uint_as_float(t[1]);
}
DI float ror8(float x) { return __uint_as_float(__builtin_amdgcn_update_dpp(0, __float_as_uint(x), 0x128, 0xF, 0xF, true)); }
DI u32 umax(u32 a, u32 b) { return a > b ? a : b; }
DI u32 max4(v4u v) { return umax(umax(v.x, v.y), umax(v.z, v.w)); }

struct Ctx {
    __amdgpu_buffer_rsrc_t r; u32 par; int h, p, wave; u32 spin;
    const u16* x; const u16* mem0; u16* out;
};
DI v4u gld(const Ctx& c, u32 off, u32 voff) { return __builtin_amdgcn_raw_buffer_load_b128(c.r, voff, amap(off, c.par), 16); }
DI void gst(const Ctx& c, u32 off, u32 voff, v4u v) { __builtin_amdgcn_raw_buffer_store_b128(v, c.r, voff, amap(off, c.par), 16); }
DI void gst64(const Ctx& c, u32 off, u32 voff, v2u v) { __builtin_amdgcn_raw_buffer_store_b64(v, c.r, voff, amap(off, c.par), 16); }
DI void gst32(const Ctx& c, u32 off, u32 voff, u32 v) { __builtin_amdgcn_raw_buffer_store_b32(v, c.r, voff, amap(off, c.par), 16); }
DI void gstv(const Ctx& c, u32 off, v4u v) { __builtin_amdgcn_raw_buffer_store_b128(v, c.r, amap(off, c.par), 0, 16); }

#define TS(c, k)
#define TSF(c, s)
#define SB() __builtin_amdgcn_sched_barrier(0)

template <int N> DI void poll(Ctx& c, const u32 (&off)[N], const u32 (&voff)[N], v4u (&v)[N]) {
    for (;;) {
        asm volatile("" ::: "memory");
        u32 m = 0;
#pragma unroll
        for (int k = 0; k < N; ++k) v[k] = gld(c, off[k], voff[k]);
#pragma unroll
        for (int k = 0; k < N; ++k) m = umax(m, max4(v[k]));
        if (__builtin_amdgcn_ballot_w64(m == SENT) == 0) return;
        if (++c.spin > SPIN_MAX) return;
    }
}

DI void probe_page(const Ctx& c, int pg) {
    u32 best[2] = {~0u, ~0u};
    const u32 o = pg * 2097152 + 1048576;
    for (int r = 0; r < 3; ++r) {
        __builtin_amdgcn_raw_buffer_store_b32(opq(SENT), c.r, 0, o, 16);
        __builtin_amdgcn_raw_buffer_store_b32(opq(SENT), c.r, 0, o + 8192, 16);
        asm volatile("s_waitcnt vmcnt(0)" ::: "memory");
#pragma unroll
        for (int k = 0; k < 2; ++k) {
            const u32 t0 = (u32)__builtin_amdgcn_s_memtime();
            const u32 v = __builtin_amdgcn_raw_buffer_load_b32(c.r, 0, o + k * 8192, 16);
            asm volatile("s_waitcnt vmcnt(0)" :: "v"(v) : "memory");
            const u32 d = (u32)__builtin_amdgcn_s_memtime() - t0;
            best[k] = d < best[k] ? d : best[k];
        }
    }
    __builtin_amdgcn_raw_buffer_store_b32(opq(best[1] < best[0] ? 1u : 0u), c.r, 0, pg * 4, 16);
}
DI void get_mask(Ctx& c) {
    const int l = lane_id();
    const u32 vo = (l < NPAGE ? l : 0) * 4;
    for (;;) {
        asm volatile("" ::: "memory");
        const u32 v = __builtin_amdgcn_raw_buffer_load_b32(c.r, vo, 0, 16);
        if (__builtin_amdgcn_ballot_w64(v == SENT) == 0 || ++c.spin > SPIN_MAX) { c.par ^= (u32)__builtin_amdgcn_ballot_w64(v == 1u); return; }
    }
}

DI void load_x(const Ctx& c, int s, int f0, int n) {
    const int l = lane_id();
    const u32 voff = (l & 15) * 1280 + (l >> 4) * 16;
    const unsigned char* b = (const unsigned char*)c.x + (size_t)s * 40960;
    for (int i = 0; i < n; ++i) { int f = f0 + i; dma(b + (f & 1) * 20480 + (f >> 1) * 64, voff, L_X + f * 1024); }
}

DI void stage_mem(Ctx& c, int s) {
    if (c.wave < 5) {
        const int l = lane_id();
        v4u v[2];
        if (s == 0) {
            const __amdgpu_buffer_rsrc_t mr = __builtin_amdgcn_make_buffer_rsrc((void*)c.mem0, 0, 10240, 0x00020000);
            const u32 m = (l & 7) * 1280 + (l >> 4) * 32 + (l & 8) / 2;
#pragma unroll
            for (int k = 0; k < 2; ++k)
#pragma unroll
                for (int j = 0; j < 4; ++j) v[k][j] = __builtin_amdgcn_raw_buffer_load_b32(mr, m + k * 128 + j * 8, c.wave * 256, 0);
        } else {
            const u32 o = OFF_MP + s * MPB + c.wave * 2048;
            const u32 off[2] = {o, o + 1024}; const u32 voff[2] = {(u32)l * 16, (u32)l * 16};
            poll<2>(c, off, voff, v);
        }
        st128(L_MEM + c.wave * 2048 + l * 16, v[0]);
        st128(L_MEM + c.wave * 2048 + 1024 + l * 16, v[1]);
    }
    barv();
}

DI void node_duty(Ctx& c, int t, int n) {
    const int l = lane_id();
    const u32 l16 = l * 16;
    { const u32 off[1] = {OFF_FU + t * 128 + c.wave * 16}; const u32 voff[1] = {0}; v4u f[1]; poll<1>(c, off, voff, f); }
    v4u u[2][4];
#pragma unroll
    for (int j = 0; j < 2; ++j)
#pragma unroll
        for (int k = 0; k < 4; ++k) u[j][k] = gld(c, uoff(t) + ((2 * n + j) * 32 + 4 * c.wave + k) * 1024, l16);
    const u32 po = OFF_MP + t * MPB + 10240, zo = zoff(t) + (2 * n * 8 + c.wave) * 1024;
    const u32 off[5] = {po + c.wave * 2048, po + c.wave * 2048 + 1024, po + 16384 + c.wave * 512, zo, zo + 8192};
    const u32 voff[5] = {l16, l16, (u32)((l >> 4) * 128 + (l & 7) * 16), l16, l16};
    v4u v[5];
    TS(c, 5);
    poll<5>(c, off, voff, v);
    TS(c, 6);
    f4 lo[2], hi[2];
#pragma unroll
    for (int j = 0; j < 2; ++j) {
        const f4 z = {0, 0, 0, 0};
        lo[j] = mfma(u[j][0], v[0], z); hi[j] = mfma(u[j][1], v[0], z);
        lo[j] = mfma(u[j][2], v[1], lo[j]); hi[j] = mfma(u[j][3], v[1], hi[j]);
        lo[j] = mfma(v[3 + j], v[2], lo[j]);
    }
    f4 r;
#pragma unroll
    for (int i = 0; i < 4; ++i) { float a = lo[0][i] + ror8(hi[0][i]), b = ror8(lo[1][i]) + hi[1][i]; r[i] = (l & 8) ? b : a; }
    st128(L_NRED + c.wave * 1024 + l16, __builtin_bit_cast(v4u, r));
    const int r16 = l >> 2, jj = l & 3;
    const u32 a = L_NRED + (jj * 16 + c.wave * 8 + (r16 & 7)) * 16 + (r16 >> 3) * 8;
    bar();
    if (c.wave < 2) {
        f2 s = lds64f(a);
#pragma unroll
        for (int w = 1; w < 8; ++w) s += lds64f(a + w * 1024);
        gst32(c, OFF_MP + (t + 1) * MPB + (2 * n + c.wave) * 256, l * 4, pkc(s.x, s.y));
    }
    TS(c, 7);
}

DI void out_duty(Ctx& c, int t, int j) {
    const int l = lane_id();
    const u32 l16 = l * 16;
    { const u32 off[2] = {OFF_FU + t * 128 + c.wave * 16, OFF_FP + t * 128 + c.wave * 16}; const u32 voff[2] = {0, 0}; v4u f[2]; poll<2>(c, off, voff, f); }
    f4 a[2] = {{0, 0, 0, 0}, {0, 0, 0, 0}};
    {
        v4u u[4], px[4][2];
#pragma unroll
        for (int k = 0; k < 4; ++k) {
            u[k] = gld(c, uoff(t) + (j * 32 + 4 * c.wave + k) * 1024, l16);
            px[k][0] = gld(c, pxoff(t) + ((4 * c.wave + k) * 2) * 1024, l16);
            px[k][1] = gld(c, pxoff(t) + ((4 * c.wave + k) * 2 + 1) * 1024, l16);
        }
#pragma unroll
        for (int k = 0; k < 4; ++k) { a[0] = mfma(u[k], px[k][0], a[0]); a[1] = mfma(u[k], px[k][1], a[1]); }
    }
    {
        const u32 po = pxoff(t) + 65536 + c.wave * 2048;
        const u32 off[3] = {zoff(t) + (j * 8 + c.wave) * 1024, po, po + 1024}; const u32 voff[3] = {l16, l16, l16};
        v4u z[3];
        poll<3>(c, off, voff, z);
        a[0] = mfma(z[0], z[1], a[0]); a[1] = mfma(z[0], z[2], a[1]);
    }
    st128(L_RED + c.wave * 2048 + l16, __builtin_bit_cast(v4u, a[0]));
    st128(L_RED + c.wave * 2048 + 1024 + l16, __builtin_bit_cast(v4u, a[1]));
    bar();
    if (c.wave < 2) {
        f4 s = __builtin_bit_cast(f4, lds128(L_RED + c.wave * 1024 + l16));
#pragma unroll
        for (int w = 1; w < 8; ++w) s += __builtin_bit_cast(f4, lds128(L_RED + (w * 2 + c.wave) * 1024 + l16));
        v2u o = {pk(s[0], s[1]), pk(s[2], s[3])};
        const __amdgpu_buffer_rsrc_t orr = __builtin_amdgcn_make_buffer_rsrc((void*)c.out, 0, 2048 * 2560 - 1280, 0x00020000);
        __builtin_amdgcn_raw_buffer_store_b64(o, orr, (l & 15) * 2560 + (l >> 4) * 8, (t * 32 + c.wave * 16) * 2560 + j * 32, 0);
    }
    bar();
}

template <bool WO> DI TB wbase(const Ctx& c, int l) {
    return mktb(WO ? 12288 + (l >> 4) * 1024 + (c.wave & 3) * 256 + rowp(l) : (c.wave >> 2) * 12288 + (l >> 4) * 3072 + (c.wave & 3) * 768 + rowp(l));
}
template <bool WO> DI void issue_chunk(const unsigned char* wb, u32 voff, int wave, int ch) {
    const u32 slot = ((ch + 2) % 6) * 24576;
#pragma unroll
    for (int u = 0; u < 3; ++u) {
        if (WO) dma(wb + (size_t)(3 * (ch & 1) + u) * 40960 + wo_col(ch >> 1) * 128, voff, slot + 12288 + (u * 4 + (wave & 3)) * 1024);
        else dma(wb + (size_t)ch * 393216 + u * 128, voff, slot + (wave >> 2) * 12288 + (wave & 3) * 3072 + u * 1024);
    }
}
template <bool WO> DI void load_weights(const Ctx& c, const u16* w, v8u (&W)[24]) {
    const int l = lane_id();
    const int kb = c.wave & 3, rr = ((l >> 1) & 7) ^ (4 * (kb & 1));
    const u32 voff = (8 * kb + rr) * (WO ? 1280 : 12288) + (l >> 4) * 32 + (l & 1) * 16;
    const unsigned char* wb = (const unsigned char*)w + (WO ? (size_t)c.h * 245760 : (size_t)c.h * 384);
    const TB wl = wbase<WO>(c, l);
#pragma unroll
    for (int i = 0; i < 5; ++i) issue_chunk<WO>(wb, voff, c.wave, i);
#pragma unroll
    for (int i = 0; i < 16; ++i) {
        asm volatile("s_waitcnt vmcnt(12) lgkmcnt(0)\n\ts_barrier" ::: "memory");
        if (i < 15) issue_chunk<WO>(wb, voff, c.wave, i + 5);
        const u32 slot = ((i + 2) % 6) * 24576;
#pragma unroll
        for (int u = 0; u < 3; ++u) {
            v4u f = trfrag(wl, slot + u * (WO ? 4096 : 256));
            const int ks = 3 * (i & 1) + u;
            const int pi = WO ? (i >> 1) * 3 + (ks >> 1) : u * 8 + (i >> 1), hf = WO ? (ks & 1) : (i & 1);
            W[pi] = hf ? cat8(lo4(W[pi]), f) : cat8(f, f);
        }
    }
    barv();
}

DI void crit_pre(TB wl, v8u (&T)[3]) {
#pragma unroll
    for (int u = 0; u < 3; ++u) T[u] = cat8(trfrag(wl, u * 256), trfrag(wl, 24576 + u * 256));
}
DI void crit_gemm1(const v8u (&W)[24], v8u (&T)[3], TB wl, int l, v2u& r01, v2u& r2) {
    const u32 idx = 0x4444u + (l & 8) * 0x1555u + ((l & 8) >> 2);
    f4 acc[3] = {{0, 0, 0, 0}, {0, 0, 0, 0}, {0, 0, 0, 0}};
    v4u a[11];
    const u32 ab = opq(L_MEM + l * 16);
    a[0] = lds128(ab + 8192);
#pragma unroll
    for (int i = 1; i < 4; ++i) a[i] = lds128(ab + (i - 1) * 1024);
    SB();
#pragma unroll
    for (int i = 0; i < 10; ++i) {
#pragma unroll
        for (int u = 0; u < 3; ++u) acc[u] = smf(a[i], (i == 0 || i == 9) ? T[u] : W[u * 8 + i - 1], acc[u], idx);
        SB();
        if (i + 4 < 10) a[i + 4] = lds128(ab + (i + 4 == 9 ? 9 : i + 3) * 1024);
        if (i >= 1 && i < 4) T[i - 1] = cat8(trfrag(wl, 49152 + (i - 1) * 256), trfrag(wl, 73728 + (i - 1) * 256));
        SB();
    }
    float s[4], t[4];
#pragma unroll
    for (int i = 0; i < 4; ++i) { s[i] = fold32(acc[0][i], acc[1][i]); t[i] = fold32(acc[2][i], acc[2][i]); }
    r01.x = pk(s[0], s[1]); r01.y = pk(s[2], s[3]); r2.x = pk(t[0], t[1]); r2.y = pk(t[2], t[3]);
}

template <class F> DI void bulk_gemm1(const Ctx& c, const v8u (&W)[24], F mid) {
    const int l = lane_id();
    const TB wl = wbase<false>(c, l);
    const u32 ab = opq(L_X + l * 16);
    f4 acc[2][3];
#pragma unroll
    for (int m = 0; m < 2; ++m)
#pragma unroll
        for (int u = 0; u < 3; ++u) acc[m][u] = f4{0, 0, 0, 0};
    v4u a[21][2], b[21][3];
    a[0][0] = lds128(ab); a[0][1] = lds128(ab + 1024);
#pragma unroll
    for (int ks = 0; ks < 20; ++ks) {
        if (ks == 11) mid(0);
        if (ks == 16) mid(1);
        if (ks < 19) { a[ks + 1][0] = lds128(ab + (ks + 1) * 2048); a[ks + 1][1] = lds128(ab + (ks + 1) * 2048 + 1024); }
        if (ks == 15) {
#pragma unroll
            for (int u = 0; u < 3; ++u) b[16][u] = trfrag(wl, u * 256);
        }
        SB();
#pragma unroll
        for (int u = 0; u < 3; ++u) {
            const v4u bb = ks < 16 ? ((ks & 1) ? hi4(W[u * 8 + (ks >> 1)]) : lo4(W[u * 8 + (ks >> 1)])) : b[ks][u];
            acc[0][u] = mfma(a[ks][0], bb, acc[0][u]); acc[1][u] = mfma(a[ks][1], bb, acc[1][u]);
            if (ks >= 16 && ks < 19) { SB(); b[ks + 1][u] = trfrag(wl, (ks - 15) * 24576 + u * 256); SB(); }
        }
        SB();
    }
    const int cc = l & 15;
    const u32 ia = L_IMG + (c.wave >> 2) * 12288 + ((c.wave & 3) * 48 + (cc ^ (4 * (cc >> 3)))) * 32 + (l >> 4) * 8;
#pragma unroll
    for (int m = 0; m < 2; ++m)
#pragma unroll
        for (int u = 0; u < 3; ++u) { v2u v = {pk(acc[m][u][0], acc[m][u][1]), pk(acc[m][u][2], acc[m][u][3])}; st64(ia + m * 6144 + u * 512, v); }
}

DI void duty(Ctx& c, int s, bool vm) {
    if (vm && c.h < 20) { if (s + 1 < NSEG - 1) node_duty(c, s + 1, c.h); }
    else if (s >= 0 && (!vm || c.h < 28)) out_duty(c, s, vm ? c.h + 12 : c.h);
}

DI void qk_main(Ctx& c, const u16* wq, const u16* wk) {
    v8u W[24];
    load_x(c, c.p, 24 + 2 * c.wave, 2);
    load_weights<false>(c, c.wave < 4 ? wq : wk, W);
    if (c.par == 0 && c.p == 1 && c.h < NPAGE) { if (c.wave == 0) probe_page(c, c.h); bar(); }
    load_x(c, c.p, 3 * c.wave, 3);
    barv();
    for (int s = c.p - 2; s < NSEG; s += 2) {
        asm volatile("" : "+s"(c.wave), "+s"(c.h), "+s"(c.par));
        if (s >= 0) {
            const int l = lane_id(), g = l >> 4, cc = l & 15;
            const TB wl = wbase<false>(c, l);
            v8u T[3];
            crit_pre(wl, T);
            TS(c, 0);
            stage_mem(c, s);
            TS(c, 1);
            v2u r01, r2;
            crit_gemm1(W, T, wl, l, r01, r2);
            const u32 fa = L_F + ((c.wave & 3) * 48 + (cc ^ (4 * (cc >> 3)))) * 32 + (c.wave >> 2) * 16 + (g & 1) * 8;
            st64(fa + (g >> 1) * 512, r01);
            if (g < 2) st64(fa + 1024, r2);
            const TB il = mktb(L_IMG + g * 256 + rowp(l));
            f4 t[3] = {{0, 0, 0, 0}, {0, 0, 0, 0}, {0, 0, 0, 0}};
            if (c.wave == 0) {
                __builtin_amdgcn_s_setprio(3);
                v4u k[6][2], f[6];
#pragma unroll
                for (int ks = 0; ks < 3; ++ks) { k[ks][0] = trfrag(il, 12288 + ks * 1024); k[ks][1] = trfrag(il, 18432 + ks * 1024); }
                bar();
                TS(c, 2);
#pragma unroll
                for (int ks = 0; ks < 4; ++ks) f[ks] = trfrag(il, L_F - L_IMG + ks * 1024);
                SB();
#pragma unroll
                for (int ks = 0; ks < 6; ++ks) {
                    t[1] = mfma(k[ks][0], f[ks], t[1]); t[2] = mfma(k[ks][1], f[ks], t[2]); t[0] = mfma(f[ks], f[ks], t[0]);
                    SB();
                    if (ks < 3) { k[ks + 3][0] = trfrag(il, 12288 + (ks + 3) * 1024); k[ks + 3][1] = trfrag(il, 18432 + (ks + 3) * 1024); }
                    if (ks < 2) f[ks + 4] = trfrag(il, L_F - L_IMG + (ks + 4) * 1024);
                    SB();
                }
            } else {
                bar();
                TS(c, 2);
                if (c.wave < 3) {
                    const TB bl = {il.a + (c.wave - 1) * 6144, il.b + (c.wave - 1) * 6144};
                    v4u k[7][4];
                    k[0][0] = trfrag(bl, 0); k[0][1] = trfrag(il, L_F - L_IMG); k[0][2] = trfrag(il, 12288); k[0][3] = trfrag(il, 18432);
#pragma unroll
                    for (int ks = 0; ks < 6; ++ks) {
                        if (ks < 5) { const u32 o = (ks + 1) * 1024; k[ks + 1][0] = trfrag(bl, o); k[ks + 1][1] = trfrag(il, L_F - L_IMG + o); k[ks + 1][2] = trfrag(il, 12288 + o); k[ks + 1][3] = trfrag(il, 18432 + o); }
                        SB();
                        t[0] = mfma(k[ks][1], k[ks][0], t[0]); t[1] = mfma(k[ks][2], k[ks][0], t[1]); t[2] = mfma(k[ks][3], k[ks][0], t[2]);
                        SB();
                    }
                } else if (s + 2 < NSEG) load_x(c, s + 2, 24 + 2 * (c.wave - 3), 2);
            }
            if (c.wave < 3) {
                const float scale = 0.07216878364870322f, l2e = 1.4426950408889634f, ninf = __uint_as_float(opq(0xFF800000u));
                const int lim = c.wave == 0 ? 64 : (c.wave - 1) * 16 + cc - 4 * g;
                float mx = ninf;
#pragma unroll
                for (int k = 0; k < 3; ++k)
#pragma unroll
                    for (int i = 0; i < 4; ++i) {
                        float v = rbf(rbf(t[k][i]) * scale);
                        const bool vis = k == 0 ? g >= 2 : (16 * (k - 1) + i <= lim);
                        v = vis ? v : ninf;
                        t[k][i] = v; mx = fmaxf(mx, v);
                    }
                mx = xmax(mx);
                float sum = 0;
#pragma unroll
                for (int k = 0; k < 3; ++k)
#pragma unroll
                    for (int i = 0; i < 4; ++i) { t[k][i] = __builtin_amdgcn_exp2f((t[k][i] - mx) * l2e); sum += t[k][i]; }
                sum = xsum(sum);
                const float inv = __builtin_amdgcn_rcpf(sum);
                const v4u px = {pkc(t[1][0] * inv, t[1][1] * inv), pkc(t[1][2] * inv, t[1][3] * inv), pkc(t[2][0] * inv, t[2][1] * inv), pkc(t[2][2] * inv, t[2][3] * inv)};
                const v2u pm = {pkc(t[0][0] * inv, t[0][1] * inv), pkc(t[0][2] * inv, t[0][3] * inv)};
                if (c.wave == 0) {
                    const u32 po = OFF_MP + s * MPB + 10240;
                    if (cc < 8) {
                        gst(c, po + (c.h >> 1) * 1024, (g * 16 + (c.h & 1) * 8 + cc) * 16, px);
                        if (g >= 2) gst64(c, po + 16384 + c.h * 128, cc * 16 + (g & 1) * 8, pm);
                    }
                    __builtin_amdgcn_s_setprio(0);
                } else {
                    const u32 po = pxoff(s);
                    gst(c, po + (c.h * 2 + c.wave - 1) * 1024, l * 16, px);
                    if (g >= 2) gst64(c, po + 65536 + ((c.h >> 2) * 2 + c.wave - 1) * 1024 + (c.h & 3) * 256, cc * 16 + (g & 1) * 8, pm);
                }
            }
            TS(c, 3);
            if (s < 2) {
                const v4u sv = sentv();
                for (int i = c.wave * 64 + lane_id(); i < 30 * 40; i += 512) {
                    const int st = c.p + 4 + 2 * (i / 40), e = i % 40;
                    const u32 o = e < 32 ? (c.h >> 1) * 1024 + ((e >> 3) * 16 + (c.h & 1) * 8 + (e & 7)) * 16 : 16384 + c.h * 128 + (e & 7) * 16;
                    gstv(c, OFF_MP + st * MPB + 10240 + o, sv);
                }
            }
        }
        auto mid = [&](int k) {
            barv();
            if (k == 0 && s >= 0) {
                if (c.wave == 7) gst32(c, OFF_FP + s * 128 + c.h * 4, 0, opq(1u));
                else if (c.wave < 6 && s + 2 < NSEG) load_x(c, s + 2, 34 + c.wave, 1);
            }
        };
        if (s + 2 < NSEG) {
            bulk_gemm1(c, W, mid);
            bar();
            if (s + 4 < NSEG) load_x(c, s + 4, 3 * c.wave, 3);
        } else mid(0);
        if (s < 0) get_mask(c);
        TS(c, 4);
        duty(c, s, false);
        TSF(c, s);
    }
}

DI void clear_z(const Ctx& c, int s) {
    const int l = lane_id();
    const v4u sv = sentv();
    for (int i = c.wave; i < 10; i += 4)
        gst(c, zoff(s) + i * 32768 + c.h * 256, (l >> 4) * 16384 + (l & 15) * 16, sv);
}
DI void flag_u(const Ctx& c, int s) { barv(); if (c.wave == 7) gst32(c, OFF_FU + s * 128 + c.h * 4, 0, opq(1u)); }

DI void v_main(Ctx& c, const u16* wv) {
    v8u W[24];
    load_x(c, c.p, 24 + 4 * c.wave, 4);
    load_weights<false>(c, wv, W);
    load_x(c, c.p, 6 * c.wave, 6);
    barv();
    for (int s = c.p - 2; s < NSEG; s += 2) {
        asm volatile("" : "+s"(c.wave), "+s"(c.h), "+s"(c.par));
        if (s >= 0) {
            const int l = lane_id(), g = l >> 4, cc = l & 15;
            const TB wl = wbase<false>(c, l);
            v8u T[3];
            crit_pre(wl, T);
            TS(c, 0);
            stage_mem(c, s);
            TS(c, 1);
            v2u r01, r2;
            crit_gemm1(W, T, wl, l, r01, r2);
            const int rho = ((cc >> 2) << 1) | (cc & 1);
            const u32 a = L_VS + ((cc >> 1) & 1) * 16 + (g & 1) * 8;
            const int t0 = c.wave * 3 + (g >> 1), t2 = c.wave * 3 + 2;
            st64(a + (t0 * 8 + (rho ^ (4 * (t0 & 1)))) * 32, r01);
            if (g < 2) st64(a + (t2 * 8 + (rho ^ (4 * (t2 & 1)))) * 32, r2);
            bar();
            TS(c, 2);
        }
        if (s + 2 < NSEG) {
            if (s >= 0) load_x(c, s + 2, 24 + 4 * c.wave, 4);
            bulk_gemm1(c, W, [&](int k) { if (k == 0) barv(); });
            bar();
            if (s + 4 < NSEG) load_x(c, s + 4, 6 * c.wave, 6);
            if (s < 0) get_mask(c);
            if (c.h < 20 && (s == 0 || s == 1)) {
                const v4u sv = sentv();
                for (int i = c.wave * 64 + lane_id(); i < 30 * 32; i += 256) {
                    const int st = 6 - c.p + 2 * (i >> 5);
                    if (st < NSEG) gstv(c, OFF_MP + st * MPB + c.h * 512 + (i & 31) * 16, sv);
                }
            }
            clear_z(c, s + 2);
            flag_u(c, s + 2);
        }
        TS(c, 4);
        if (s >= -1) duty(c, s, true);
        TSF(c, s);
    }
}

DI u32 wo_off(int i, int ks) { return ((i - 8) * 2 + ks / 3) * 24576 + (ks % 3) * 4096; }
DI void bulk_gemm2(const Ctx& c, const v8u (&W)[24], int s) {
    const int l = lane_id();
    const TB wl = wbase<true>(c, l), il = mktb(L_IMG + (l >> 4) * 256 + rowp(l));
    v4u a[19][2], b[19];
    a[0][0] = trfrag(il, 0); a[0][1] = trfrag(il, 6144);
#pragma unroll
    for (int ig = 0; ig < 3; ++ig) {
        const int nt = ig == 0 ? 4 : 3;
        f4 acc[2][4];
#pragma unroll
        for (int j = 0; j < 4; ++j) { acc[0][j] = f4{0, 0, 0, 0}; acc[1][j] = f4{0, 0, 0, 0}; }
#pragma unroll
        for (int ks = 0; ks < 6; ++ks) {
            const int st = ig * 6 + ks, n1 = (st + 1) % 6;
            if (st < 17) { a[st + 1][0] = trfrag(il, n1 * 1024); a[st + 1][1] = trfrag(il, 6144 + n1 * 1024); }
            if (st >= 5 && st < 17) b[st + 1] = trfrag(wl, wo_off(7 + (st + 1) / 6, n1));
            SB();
#pragma unroll
            for (int j = 0; j < nt; ++j) {
                const int i = ig == 0 ? j : (j < 2 ? 2 + 2 * ig + j : 7 + ig);
                const v4u bb = i < 8 ? ((ks & 1) ? hi4(W[i * 3 + (ks >> 1)]) : lo4(W[i * 3 + (ks >> 1)])) : b[st];
                acc[0][j] = mfma(a[st][0], bb, acc[0][j]); acc[1][j] = mfma(a[st][1], bb, acc[1][j]);
            }
            SB();
        }
#pragma unroll
        for (int j = 0; j < nt; ++j) {
            const int i = ig == 0 ? j : (j < 2 ? 2 + 2 * ig + j : 7 + ig);
            const v4u v = {pk(acc[0][j][0], acc[0][j][1]), pk(acc[0][j][2], acc[0][j][3]), pk(acc[1][j][0], acc[1][j][1]), pk(acc[1][j][2], acc[1][j][3])};
            gst(c, uoff(s) + ((wo_col(i) * 4 + (c.wave & 3)) * 32 + c.h) * 1024, l * 16, v);
        }
        SB();
    }
}

DI void o_main(Ctx& c, const u16* wo) {
    v8u W[24];
    load_weights<true>(c, wo, W);
    barv();
    for (int s = c.p - 2; s < NSEG; s += 2) {
        asm volatile("" : "+s"(c.wave), "+s"(c.h), "+s"(c.par));
        if (s >= 0) {
            const int l = lane_id(), g = l >> 4;
            const TB wl = wbase<true>(c, l), il = mktb(L_VS + g * 256 + rowp(l));
            const u32 idx = 0x4444u + (l & 8) * 0x1555u + ((l & 8) >> 2);
            const u32 za = ((g >> 1) + 2 * (g & 1)) * 65536 + (l & 15) * 16;
            stage_mem(c, s);
            v8u T[3];
#pragma unroll
            for (int f = 0; f < 3; ++f) T[f] = cat8(trfrag(wl, wo_off(8, 2 * f)), trfrag(wl, wo_off(8, 2 * f + 1)));
            __builtin_amdgcn_s_setprio(3);
            bar();
            TS(c, 2);
            v4u a[3];
#pragma unroll
            for (int f = 0; f < 3; ++f) a[f] = trfrag(il, f * 1024);
            SB();
            f4 acc[6][2];
            u32 xp[5][2];
#pragma unroll
            for (int pr = 0; pr < 6; ++pr) {
                if (pr < 5) {
                    acc[pr][0] = f4{0, 0, 0, 0}; acc[pr][1] = f4{0, 0, 0, 0};
#pragma unroll
                    for (int f = 0; f < 3; ++f)
#pragma unroll
                        for (int j = 0; j < 2; ++j) {
                            const int o = 2 * pr + j, i = o == 0 ? 8 : (o == 4 ? 9 : (o < 4 ? o - 1 : o - 2));
                            acc[pr][j] = smf(a[f], i < 8 ? W[i * 3 + f] : T[f], acc[pr][j], idx);
                        }
                }
                if (pr == 0) {
                    SB();
#pragma unroll
                    for (int f = 0; f < 3; ++f) T[f] = cat8(trfrag(wl, wo_off(9, 2 * f)), trfrag(wl, wo_off(9, 2 * f + 1)));
                } else {
                    const int k = pr - 1;
                    float x[4];
#pragma unroll
                    for (int i = 0; i < 4; ++i) x[i] = fold32(acc[k][0][i], acc[k][1][i]);
                    xp[k][0] = pkc(x[0], x[1]); xp[k][1] = pkc(x[2], x[3]);
                    if (k == 1 || k == 3 || k == 4) {
                        const int k0 = k == 4 ? 4 : k - 1;
                        auto s0 = __builtin_amdgcn_permlane16_swap(xp[k0][0], xp[k][0], false, false);
                        auto s1 = __builtin_amdgcn_permlane16_swap(xp[k0][1], xp[k][1], false, false);
                        const v4u v = {s0[0], s1[0], s0[1], s1[1]};
                        if (k < 4 || !(g & 1)) gst(c, zoff(s) + (k >> 1) * 131072 + (c.wave & 3) * 8192 + c.h * 256, za, v);
                    }
                }
                SB();
            }
            __builtin_amdgcn_s_setprio(0);
            TS(c, 3);
        }
        if (s + 2 < NSEG) {
            bar();
            if (s < 0) get_mask(c);
            bar();
            bulk_gemm2(c, W, s + 2);
            flag_u(c, s + 2);
        }
        TS(c, 4);
        if (s >= -1) duty(c, s, true);
        TSF(c, s);
    }
}

__global__ void __launch_bounds__(512, 2) memtr_kernel(Args a) {
    Ctx c;
    const int wg = blockIdx.x;
    c.wave = __builtin_amdgcn_readfirstlane(threadIdx.x >> 6);
    const int br = (wg & 7) >> 2;
    const int idx = (wg >> 3) * 4 + (wg & 3);
    c.h = idx & 31; c.p = idx >> 6;
    c.r = __builtin_amdgcn_make_buffer_rsrc(a.ws, 0, WS_SPAN, 0x00020000);
    c.par = br ? ~0u : 0u;
    c.spin = 0;
    if (((size_t)a.ws & 0x1FFFFF) != 0) { const unsigned long long t0 = __builtin_amdgcn_s_memrealtime(); while (__builtin_amdgcn_s_memrealtime() - t0 < ((((size_t)a.ws & 0x1FFF) != 0) ? 40000 : 20000)) __builtin_amdgcn_s_sleep(10); }
    c.x = br ? a.x[1] : a.x[0]; c.mem0 = br ? a.mem0[1] : a.mem0[0]; c.out = a.out + br * 640;
    if (((idx >> 5) & 1) == 0) qk_main(c, br ? a.w[1][0] : a.w[0][0], br ? a.w[1][1] : a.w[0][1]);
    else if (c.wave < 4) v_main(c, br ? a.w[1][2] : a.w[0][2]);
    else o_main(c, br ? a.w[1][3] : a.w[0][3]);
}

extern "C" void kernel_launch(void* const* d_in, const int* in_sizes, int n_in, void* d_out, int out_size, void* d_ws, size_t ws_size, hipStream_t stream) {
    Args a;
    for (int b = 0; b < 2; ++b) {
        a.x[b] = (const u16*)d_in[7 * b]; a.mem0[b] = (const u16*)d_in[7 * b + 1];
        for (int k = 0; k < 4; ++k) a.w[b][k] = (const u16*)d_in[7 * b + 2 + k];
    }
    a.out = (u16*)d_out; a.ws = (unsigned char*)d_ws;
    (void)hipMemsetAsync(d_ws, 0xFF, INIT_BYTES, stream);
    memtr_kernel<<<256, 512, 0, stream>>>(a);
}
```

```cpp
#include <hip/hip_runtime.h>
#include <stdint.h>

typedef unsigned u32;
typedef unsigned short u16;
typedef u32 v2u __attribute__((ext_vector_type(2)));
typedef u32 v4u __attribute__((ext_vector_type(4)));
typedef u32 v8u __attribute__((ext_vector_type(8)));
typedef float f4 __attribute__((ext_vector_type(4)));
typedef float f2 __attribute__((ext_vector_type(2)));
typedef short s4 __attribute__((ext_vector_type(4)));
typedef __bf16 b2 __attribute__((ext_vector_type(2)));
typedef __bf16 b8 __attribute__((ext_vector_type(8)));
typedef __bf16 b16 __attribute__((ext_vector_type(16)));
#define AS3 __attribute__((address_space(3)))
#define DI __device__ __forceinline__

constexpr int NSEG = 64;
constexpr u32 SENT = 0xFFFFFFFFu;
constexpr int NPAGE = 13;
constexpr u32 WS_SPAN = NPAGE * 2097152;
constexpr u32 INIT_OFF = 2097152 - 147456, INIT_BYTES = 147456 + 5 * 65536;
constexpr u32 MP_PM = 20480, MP_PX = 32768;
constexpr u32 L_IMG = 98304, L_X = 122880, L_MEM = L_X + 24576, L_F = L_X + 34816;
constexpr u32 V_MEM = L_IMG + 12288, V_VS = V_MEM + 10240, V_DUMP = V_VS + 3072, V_X = V_DUMP + 1024;
#define QSLEEP 5
#define UPRIO 2
#define NSLEEP 8
#define VSLEEP 16
constexpr u32 SPIN_MAX = 1u << 20;

struct Args { const u16* x[2]; const u16* mem0[2]; const u16* w[2][4]; u16* out; unsigned char* ws; };

__shared__ __attribute__((aligned(1024))) unsigned char lds[163840];

DI constexpr int wo_col(int i) { return i == 8 ? 0 : (i == 9 ? 4 : (i < 3 ? i + 1 : i + 2)); }
DI v4u lds128(u32 a) { return *(AS3 v4u*)((AS3 unsigned char*)lds + a); }
DI f2 lds64f(u32 a) { return *(AS3 f2*)((AS3 unsigned char*)lds + a); }
DI void st128(u32 a, v4u v) { *(AS3 v4u*)((AS3 unsigned char*)lds + a) = v; }
DI void st64(u32 a, v2u v) { *(AS3 v2u*)((AS3 unsigned char*)lds + a) = v; }
DI void st32(u32 a, u32 v) { *(AS3 u32*)((AS3 unsigned char*)lds + a) = v; }
DI v2u ldtr(u32 a) { return __builtin_bit_cast(v2u, __builtin_amdgcn_ds_read_tr16_b64_v4i16((AS3 s4*)((AS3 unsigned char*)lds + a))); }
struct TB { u32 a, b; };
DI u32 opq(u32 v);
DI TB mktb(u32 a) { TB t = {opq(a), opq(a ^ 128u)}; return t; }
DI v4u trfrag(TB t, u32 off) { v2u lo = ldtr(t.a + off), hi = ldtr(t.b + off); v4u r = {lo.x, lo.y, hi.x, hi.y}; return r; }
DI int lane_id() { int l; asm volatile("v_mbcnt_lo_u32_b32 %0, -1, 0\n\tv_mbcnt_hi_u32_b32 %0, -1, %0" : "=v"(l)); return l; }
DI u32 opq(u32 v) { asm volatile("" : "+v"(v)); return v; }
DI v4u sentv() { const u32 v = opq(SENT); v4u r = {v, v, v, v}; return r; }
DI u32 rowp(int l) { return ((((l & 15) >> 2) ^ ((l >> 2) & 4)) * 32 + (l & 3) * 8); }
DI v8u cat8(v4u a, v4u b) { return __builtin_shufflevector(a, b, 0, 1, 2, 3, 4, 5, 6, 7); }
DI v4u lo4(v8u a) { return __builtin_shufflevector(a, a, 0, 1, 2, 3); }
DI v4u hi4(v8u a) { return __builtin_shufflevector(a, a, 4, 5, 6, 7); }
DI f4 mfma(v4u a, v4u b, f4 c) { return __builtin_amdgcn_mfma_f32_16x16x32_bf16(__builtin_bit_cast(b8, a), __builtin_bit_cast(b8, b), c, 0, 0, 0); }
DI f4 smf(v4u a, v8u b, f4 c, u32 idx) { return __builtin_amdgcn_smfmac_f32_16x16x64_bf16(__builtin_bit_cast(b8, a), __builtin_bit_cast(b16, b), c, (int)idx, 0, 0); }
DI u32 pk(float a, float b) { b2 r = {(__bf16)a, (__bf16)b}; return __builtin_bit_cast(u32, r); }
DI u32 pkc(float a, float b) { u32 r = pk(a, b); return r < 0xFFFFFFFEu ? r : 0xFFFFFFFEu; }
DI void bar() { asm volatile("s_waitcnt lgkmcnt(0)\n\ts_barrier" ::: "memory"); }
DI void barv() { asm volatile("s_waitcnt vmcnt(0) lgkmcnt(0)\n\ts_barrier" ::: "memory"); }
DI void dma(const void* sbase, u32 voff, u32 ldst) {
    asm volatile("s_mov_b32 m0, %2\n\ts_nop 0\n\tglobal_load_lds_dwordx4 %0, %1" :: "v"(voff), "s"(sbase), "s"(ldst) : "memory");
}
template <int GO, int LO> DI void dmai(const void* sbase, u32 voff, u32 ldst) {
    asm volatile("s_add_u32 m0, %2, %3\n\ts_nop 0\n\tglobal_load_lds_dwordx4 %0, %1 offset:%4" :: "v"(voff), "s"(sbase), "s"(ldst), "n"(LO - GO), "n"(GO) : "memory", "scc");
}
DI float fold32(float a, float b) { auto s = __builtin_amdgcn_permlane32_swap(__float_as_uint(a), __float_as_uint(b), false, false); return __uint_as_float(s[0]) + __uint_as_float(s[1]); }
DI float xmax(float x) {
    auto s = __builtin_amdgcn_permlane16_swap(__float_as_uint(x), __float_as_uint(x), false, false); x = fmaxf(__uint_as_float(s[0]), __uint_as_float(s[1]));
    auto t = __builtin_amdgcn_permlane32_swap(__float_as_uint(x), __float_as_uint(x), false, false); return fmaxf(__uint_as_float(t[0]), __uint_as_float(t[1]));
}
DI float xsum(float x) {
    auto s = __builtin_amdgcn_permlane16_swap(__float_as_uint(x), __float_as_uint(x), false, false); x = __uint_as_float(s[0]) + __uint_as_float(s[1]);
    auto t = __builtin_amdgcn_permlane32_swap(__float_as_uint(x), __float_as_uint(x), false, false); return __uint_as_float(t[0]) + __uint_as_float(t[1]);
}
DI float ror8(float x) { return __uint_as_float(__builtin_amdgcn_update_dpp(0, __float_as_uint(x), 0x128, 0xF, 0xF, true)); }
DI u32 umax(u32 a, u32 b) { return a > b ? a : b; }
DI u32 max4(v4u v) { return umax(umax(v.x, v.y), umax(v.z, v.w)); }

struct Ctx {
    __amdgpu_buffer_rsrc_t r; u32 par; int h, p, wave; u32 spin;
    const u16* x; const u16* mem0; u16* out;
};
DI constexpr u32 io(u32 o) { return ((o >> 13) << 14) | (o & 8191u); }
DI u32 pgb(const Ctx& c, u32 page) { return (page << 21) | (((c.par >> page) & 1u) << 13); }
DI u32 mpb(const Ctx& c, int s) { return pgb(c, 1 + ((u32)s >> 5)) + ((u32)s & 31u) * 65536u; }
DI u32 zb(const Ctx& c, int s) { const u32 q = (u32)s / 3u; return pgb(c, 3 + (q & 1u)) + ((u32)s - 3u * q) * 655360u; }
DI u32 ub(const Ctx& c, int s, u32 w) { const u32 b = ((u32)s % 6u) * 4u + w, q = b / 3u; return pgb(c, 5 + q) + (b - 3u * q) * 655360u; }
DI u32 pxb(const Ctx& c, int s) { return pgb(c, 0) + ((u32)s % 6u) * 163840u; }
DI u32 fb(const Ctx& c, int s) { return pgb(c, 0) + (INIT_OFF + 16384) + io((u32)s * 1024u); }
DI v4u gld(const Ctx& c, u32 off, u32 voff) { return __builtin_amdgcn_raw_buffer_load_b128(c.r, voff, off, 16); }
DI void gst(const Ctx& c, u32 off, u32 voff, v4u v) { __builtin_amdgcn_raw_buffer_store_b128(v, c.r, voff, off, 16); }
DI void gst64(const Ctx& c, u32 off, u32 voff, v2u v) { __builtin_amdgcn_raw_buffer_store_b64(v, c.r, voff, off, 16); }
DI void gst32(const Ctx& c, u32 off, u32 voff, u32 v) { __builtin_amdgcn_raw_buffer_store_b32(v, c.r, voff, off, 16); }

#define TS(c, k)
#define TSF(c, s)
#define SB() __builtin_amdgcn_sched_barrier(0)

template <int N> DI void poll(Ctx& c, const u32 (&off)[N], const u32 (&voff)[N], v4u (&v)[N]) {
    for (;;) {
        asm volatile("" ::: "memory");
        u32 m = 0;
#pragma unroll
        for (int k = 0; k < N; ++k) v[k] = gld(c, off[k], voff[k]);
#pragma unroll
        for (int k = 0; k < N; ++k) m = umax(m, max4(v[k]));
        if (__builtin_amdgcn_ballot_w64(m == SENT) == 0) return;
        if (++c.spin > SPIN_MAX) return;
    }
}

DI void probe_page(const Ctx& c, int pg) {
    u32 best[2] = {~0u, ~0u};
    const u32 o = pg * 2097152 + 1048576;
    for (int r = 0; r < 3; ++r) {
        __builtin_amdgcn_raw_buffer_store_b32(opq(SENT), c.r, 0, o, 16);
        __builtin_amdgcn_raw_buffer_store_b32(opq(SENT), c.r, 0, o + 8192, 16);
        asm volatile("s_waitcnt vmcnt(0)" ::: "memory");
#pragma unroll
        for (int k = 0; k < 2; ++k) {
            const u32 t0 = (u32)__builtin_amdgcn_s_memtime();
            const u32 v = __builtin_amdgcn_raw_buffer_load_b32(c.r, 0, o + k * 8192, 16);
            asm volatile("s_waitcnt vmcnt(0)" :: "v"(v) : "memory");
            const u32 d = (u32)__builtin_amdgcn_s_memtime() - t0;
            best[k] = d < best[k] ? d : best[k];
        }
    }
    __builtin_amdgcn_raw_buffer_store_b32(opq(best[1] < best[0] ? 1u : 0u), c.r, 0, INIT_OFF + pg * 4, 16);
}
DI void get_mask(Ctx& c) {
    const int l = lane_id();
    const u32 vo = (l < NPAGE ? l : 0) * 4;
    for (;;) {
        asm volatile("" ::: "memory");
        const u32 v = __builtin_amdgcn_raw_buffer_load_b32(c.r, vo, INIT_OFF, 16);
        if (__builtin_amdgcn_ballot_w64(v == SENT) == 0 || ++c.spin > SPIN_MAX) { c.par ^= (u32)__builtin_amdgcn_ballot_w64(v == 1u); return; }
    }
}

template <int N, int ST, int I = 0> DI void dma_x(const unsigned char* b, u32 voff, u32 dst) {
    if constexpr (I < N) { dmai<I * ST * 64, I * ST * 2048>(b, voff, dst); dma_x<N, ST, I + 1>(b, voff, dst); }
}
template <int N, int ST> DI void load_x(const Ctx& c, int s, int ks, int m, u32 dst) {
    const int l = lane_id();
    dma_x<N, ST>((const unsigned char*)c.x + (s * 40960 + m * 20480 + ks * 64), (l >> 2) * 1280 + (((l & 3) ^ (-(l >> 4) & 3)) << 4), dst + ks * 2048 + m * 1024);
}
DI u32 xfrag(int l) { return (l & 15) * 64 + (((l >> 4) ^ (-(l >> 2) & 3)) << 4); }
constexpr u32 V_XH = V_X - 16 * 2048, V_XL = V_X + 4 * 2048;
DI void prefetch_x(const Ctx& c, int s, u32 dump) {
    const unsigned char* b = (const unsigned char*)c.x + (s * 40960 + c.wave * 5120);
    const u32 voff = lane_id() * 16;
    dmai<0, 0>(b, voff, dump); dmai<1024, 0>(b, voff, dump); dmai<2048, 0>(b, voff, dump); dmai<3072, 0>(b, voff, dump); dmai<0, 0>(b + 4096, voff, dump);
}
DI u32 flag_load(const Ctx& c, int s, bool vm) {
    const int l = lane_id();
    const bool node = vm && c.h < 20;
    const u32 w = node ? (c.h & 1) * 2 : (c.h & 3), d = node ? 128 : 512 - w * 128;
    return __builtin_amdgcn_raw_buffer_load_b32(c.r, (l & 3) * 4 + ((l & 4) ? d : 0), fb(c, s < 0 ? 0 : s) + w * 128 + c.wave * 16, 16);
}
DI u32 pending(u32 v) { u32 r = (u32)__builtin_amdgcn_ballot_w64(v == SENT); asm volatile("" : "+s"(r)); return r; }
DI void flag_wait(Ctx& c, int s, bool vm, u32 miss) {
    while (miss != 0 && ++c.spin < SPIN_MAX) { asm volatile("" ::: "memory"); miss = pending(flag_load(c, s, vm)); }
    asm volatile("" ::: "memory");
}

template <u32 LM> DI void stage_mem(Ctx& c, int s) {
    if (c.wave < 5) {
        const int l = lane_id();
        v4u v[2];
        if (s == 0) {
            const __amdgpu_buffer_rsrc_t mr = __builtin_amdgcn_make_buffer_rsrc((void*)c.mem0, 0, 10240, 0x00020000);
            const u32 m = (l & 7) * 1280 + (l >> 4) * 32 + (l & 8) / 2;
#pragma unroll
            for (int k = 0; k < 2; ++k)
#pragma unroll
                for (int j = 0; j < 4; ++j) v[k][j] = __builtin_amdgcn_raw_buffer_load_b32(mr, m + k * 128 + j * 8, c.wave * 256, 0);
        } else {
            const u32 o = mpb(c, s) + io(c.wave * 2048);
            const u32 off[2] = {o, o}; const u32 voff[2] = {(u32)l * 16, (u32)l * 16 + 1024};
            poll<2>(c, off, voff, v);
        }
        st128(LM + c.wave * 2048 + l * 16, v[0]);
        st128(LM + c.wave * 2048 + 1024 + l * 16, v[1]);
    }
    barv();
}

DI void node_duty(Ctx& c, int t, int n) {
    const int l = lane_id();
    const u32 l16 = l * 16;
    v4u u[2][4];
#pragma unroll
    for (int j = 0; j < 2; ++j)
#pragma unroll
        for (int k = 0; k < 4; ++k) u[j][k] = gld(c, ub(c, t, (2 * n + j) & 3) + (n >> 1) * 65536 + io(c.wave * 4096), l16 + k * 1024);
    const u32 po = mpb(c, t), zo = zb(c, t) + n * 32768 + c.wave * 1024, mo = mpb(c, t + 1) + io((2 * n + (c.wave & 1)) * 256);
    const u32 off[3] = {po + MP_PX + io(c.wave * 2048), po + MP_PX + io(c.wave * 2048), po + MP_PM + c.wave * 512}, zoff[2] = {zo, zo + 16384};
    const u32 voff[3] = {l16, l16 + 1024, (u32)((l >> 4) * 128 + (l & 7) * 16)}, zvoff[2] = {l16, l16};
    v4u v[3], zz[2];
    const int r16 = l >> 2, jj = l & 3;
    const u32 a = opq(V_MEM + (jj * 16 + c.wave * 8 + (r16 & 7)) * 16 + (r16 >> 3) * 8), ra = opq(V_MEM + c.wave * 1024 + l16);
    asm volatile("" :: "s"(mo));
    if (c.wave < 4) __builtin_amdgcn_s_sleep(NSLEEP);
    TS(c, 5);
    poll<2>(c, zoff, zvoff, zz);
    poll<3>(c, off, voff, v);
    TS(c, 6);
    f4 lo[2], hi[2];
#pragma unroll
    for (int j = 0; j < 2; ++j) {
        const f4 z = {0, 0, 0, 0};
        lo[j] = mfma(u[j][0], v[0], z); hi[j] = mfma(u[j][1], v[0], z);
        lo[j] = mfma(u[j][2], v[1], lo[j]); hi[j] = mfma(u[j][3], v[1], hi[j]);
        lo[j] = mfma(zz[j], v[2], lo[j]);
    }
    f4 r;
#pragma unroll
    for (int i = 0; i < 4; ++i) { float a = lo[0][i] + ror8(hi[0][i]), b = ror8(lo[1][i]) + hi[1][i]; r[i] = (l & 8) ? b : a; }
    st128(ra, __builtin_bit_cast(v4u, r));
    bar();
    if (c.wave < 2) {
        f2 s = lds64f(a);
#pragma unroll
        for (int w = 1; w < 8; ++w) s += lds64f(a + w * 1024);
        gst32(c, mo, l * 4, pkc(s.x, s.y));
    } else __builtin_amdgcn_s_sleep(3);
    TS(c, 7);
}

template <u32 LR, class F> DI void out_duty(Ctx& c, int t, int j, F mid) {
    const int l = lane_id();
    const u32 l16 = l * 16;
    f4 a[2] = {{0, 0, 0, 0}, {0, 0, 0, 0}};
    v4u u[4], px[4][2], z[3];
#pragma unroll
    for (int k = 0; k < 4; ++k) {
        u[k] = gld(c, ub(c, t, j & 3) + (j >> 2) * 65536 + io(c.wave * 4096), l16 + k * 1024);
        px[k][0] = gld(c, pxb(c, t) + c.wave * 16384 + (k >> 1) * 4096, l16 + (k & 1) * 2048);
        px[k][1] = gld(c, pxb(c, t) + c.wave * 16384 + (k >> 1) * 4096, l16 + (k & 1) * 2048 + 1024);
    }
    const u32 po = pxb(c, t) + 131072 + io(c.wave * 2048);
    const u32 off[1] = {zb(c, t) + j * 16384 + c.wave * 1024}; const u32 voff[1] = {l16};
    SB();
    a[0] = mfma(u[0], px[0][0], a[0]); a[1] = mfma(u[0], px[0][1], a[1]);
    SB();
    z[0] = gld(c, off[0], l16); z[1] = gld(c, po, l16); z[2] = gld(c, po, l16 + 1024);
    SB();
#pragma unroll
    for (int k = 1; k < 4; ++k) { a[0] = mfma(u[k], px[k][0], a[0]); a[1] = mfma(u[k], px[k][1], a[1]); }
    SB();
    if (__builtin_amdgcn_ballot_w64(max4(z[0]) == SENT) != 0) { v4u zz[1]; poll<1>(c, off, voff, zz); z[0] = zz[0]; }
    a[0] = mfma(z[0], z[1], a[0]); a[1] = mfma(z[0], z[2], a[1]);
    mid();
    if (c.wave != 0) st128(LR + (c.wave - 1) * 1024 + l16, __builtin_bit_cast(v4u, a[0]));
    if (c.wave != 1) st128(LR + 7168 + (c.wave ? c.wave - 1 : 0) * 1024 + l16, __builtin_bit_cast(v4u, a[1]));
    bar();
    if (c.wave < 2) {
        f4 s = c.wave ? a[1] : a[0];
#pragma unroll
        for (int w = 0; w < 7; ++w) s += __builtin_bit_cast(f4, lds128(LR + c.wave * 7168 + w * 1024 + l16));
        v2u o = {pk(s[0], s[1]), pk(s[2], s[3])};
        const __amdgpu_buffer_rsrc_t orr = __builtin_amdgcn_make_buffer_rsrc((void*)c.out, 0, 2048 * 2560 - 1280, 0x00020000);
        __builtin_amdgcn_raw_buffer_store_b64(o, orr, (l & 15) * 2560 + (l >> 4) * 8, (t * 32 + c.wave * 16) * 2560 + j * 32, 0);
    }
    bar();
}

template <bool WO> DI TB wbase(const Ctx& c, int l) {
    return mktb(WO ? 12288 + (l >> 4) * 1024 + (c.wave & 3) * 256 + rowp(l) : (c.wave >> 2) * 12288 + (l >> 4) * 3072 + (c.wave & 3) * 768 + rowp(l));
}
template <bool WO> DI void issue_chunk(const unsigned char* wb, u32 voff, int wave, int ch) {
    const u32 slot = ((ch + 2) % 6) * 24576;
#pragma unroll
    for (int u = 0; u < 3; ++u) {
        if (WO) dma(wb + (size_t)(3 * (ch & 1) + u) * 40960 + wo_col(ch >> 1) * 128, voff, slot + 12288 + (u * 4 + (wave & 3)) * 1024);
        else dma(wb + (size_t)ch * 393216 + u * 128, voff, slot + (wave >> 2) * 12288 + (wave & 3) * 3072 + u * 1024);
    }
}
template <bool WO> DI void load_weights(const Ctx& c, const u16* w, v8u (&W)[24]) {
    const int l = lane_id();
    const int kb = c.wave & 3, rr = ((l >> 1) & 7) ^ (4 * (kb & 1));
    const u32 voff = (8 * kb + rr) * (WO ? 1280 : 12288) + (l >> 4) * 32 + (l & 1) * 16;
    const unsigned char* wb = (const unsigned char*)w + (WO ? (size_t)c.h * 245760 : (size_t)c.h * 384);
    const TB wl = wbase<WO>(c, l);
#pragma unroll
    for (int i = 0; i < 5; ++i) issue_chunk<WO>(wb, voff, c.wave, i);
#pragma unroll
    for (int i = 0; i < 16; ++i) {
        asm volatile("s_waitcnt vmcnt(12) lgkmcnt(0)\n\ts_barrier" ::: "memory");
        if (i < 15) issue_chunk<WO>(wb, voff, c.wave, i + 5);
        const u32 slot = ((i + 2) % 6) * 24576;
#pragma unroll
        for (int u = 0; u < 3; ++u) {
            v4u f = trfrag(wl, slot + u * (WO ? 4096 : 256));
            const int ks = 3 * (i & 1) + u;
            const int pi = WO ? (i >> 1) * 3 + (ks >> 1) : u * 8 + (i >> 1), hf = WO ? (ks & 1) : (i & 1);
            W[pi] = hf ? cat8(lo4(W[pi]), f) : cat8(f, f);
        }
    }
    barv();
}

template <u32 LM> DI void crit_gemm1(Ctx& c, int s, const v8u (&W)[24], int l, u32 d01, u32 d2) {
    TB wl = wbase<false>(c, l);
    v8u T[3];
#pragma unroll
    for (int u = 0; u < 3; ++u) T[u] = cat8(trfrag(wl, u * 256), trfrag(wl, 24576 + u * 256));
    wl.a = opq(wl.a + 49152); wl.b = opq(wl.b + 49152);
    const u32 idx = opq(0x4444u + (l & 8) * 0x1555u + ((l & 8) >> 2)), ab = opq(LM + l * 16);
    d01 = opq(d01); d2 = opq(d2);
    f4 acc[3] = {{0, 0, 0, 0}, {0, 0, 0, 0}, {0, 0, 0, 0}};
    asm volatile("" : "+v"(acc[0]), "+v"(acc[1]), "+v"(acc[2]));
    TS(c, 0);
    stage_mem<LM>(c, s);
    TS(c, 1);
    v4u a[11];
    a[0] = lds128(ab + 8192);
    SB();
#pragma unroll
    for (int i = 1; i < 4; ++i) a[i] = lds128(ab + (i - 1) * 1024);
    SB();
#pragma unroll
    for (int i = 0; i < 10; ++i) {
#pragma unroll
        for (int u = 0; u < 3; ++u) acc[u] = smf(a[i], (i == 0 || i == 9) ? T[u] : W[u * 8 + i - 1], acc[u], idx);
        SB();
        if (i + 4 < 10) a[i + 4] = lds128(ab + (i + 4 == 9 ? 9 : i + 3) * 1024);
        if (i >= 3 && i < 6) T[i - 3] = cat8(trfrag(wl, (i - 3) * 256), trfrag(wl, 24576 + (i - 3) * 256));
        SB();
    }
    float r[4];
#pragma unroll
    for (int i = 0; i < 4; ++i) r[i] = fold32(acc[0][i], acc[1][i]);
    st64(d01, v2u{pk(r[0], r[1]), pk(r[2], r[3])});
    st32(d2, pk(fold32(acc[2][0], acc[2][2]), fold32(acc[2][1], acc[2][3])));
}

template <class F> DI void bulk_gemm1(const Ctx& c, const v8u (&W)[24], F mid) {
    const int l = lane_id();
    const TB wl = wbase<false>(c, l);
    const u32 ab = opq(L_X + xfrag(l));
    f4 acc[2][3];
#pragma unroll
    for (int m = 0; m < 2; ++m)
#pragma unroll
        for (int u = 0; u < 3; ++u) acc[m][u] = f4{0, 0, 0, 0};
    v4u a[22][2], b[21][3];
#pragma unroll
    for (int i = 0; i < 4; ++i) a[i >> 1][i & 1] = lds128(ab + i * 1024);
#pragma unroll
    for (int ks = 0; ks < 20; ++ks) {
        if (ks == 11 || ks == 16) { mid(ks == 16); a[ks + 1][0] = lds128(ab + (ks + 1) * 2048); a[ks + 1][1] = lds128(ab + (ks + 1) * 2048 + 1024); }
        const bool pf = ks < 18 && ks != 10 && ks != 15;
        SB();
        if (ks < 16) {
#pragma unroll
            for (int m = 0; m < 2; ++m) {
#pragma unroll
                for (int u = 0; u < 3; ++u) acc[m][u] = mfma(a[ks][m], (ks & 1) ? hi4(W[u * 8 + (ks >> 1)]) : lo4(W[u * 8 + (ks >> 1)]), acc[m][u]);
                SB();
                if (pf) a[ks + 2][m] = lds128(ab + (ks + 2) * 2048 + m * 1024);
                if (ks == 15 && m == 0) {
#pragma unroll
                    for (int u = 0; u < 3; ++u) b[16][u] = trfrag(wl, u * 256);
                }
                SB();
            }
        } else {
#pragma unroll
            for (int u = 0; u < 3; ++u) {
                acc[0][u] = mfma(a[ks][0], b[ks][u], acc[0][u]); acc[1][u] = mfma(a[ks][1], b[ks][u], acc[1][u]);
                if (ks < 19) { SB(); b[ks + 1][u] = trfrag(wl, (ks - 15) * 24576 + u * 256); SB(); }
            }
            if (pf) { a[ks + 2][0] = lds128(ab + (ks + 2) * 2048); a[ks + 2][1] = lds128(ab + (ks + 2) * 2048 + 1024); }
            SB();
        }
    }
    mid(2);
    const int cc = l & 15;
    const u32 ia = L_IMG + (c.wave >> 2) * 12288 + ((c.wave & 3) * 48 + (cc ^ (4 * (cc >> 3)))) * 32 + (l >> 4) * 8;
#pragma unroll
    for (int m = 0; m < 2; ++m)
#pragma unroll
        for (int u = 0; u < 3; ++u) { v2u v = {pk(acc[m][u][0], acc[m][u][1]), pk(acc[m][u][2], acc[m][u][3])}; st64(ia + m * 6144 + u * 512, v); }
}

DI void vm_duty(Ctx& c, int s, u32 fl) {
    auto ldx = [&] {
        if (s + 6 < NSEG) {
            load_x<1, 1>(c, s + 6, 16 + (c.wave >> 1), c.wave & 1, V_XH);
            load_x<3, 4>(c, s + 6, c.wave >> 1, c.wave & 1, V_XL);
            if (c.wave < 6) load_x<1, 1>(c, s + 6, 12 + (c.wave >> 1), c.wave & 1, V_XL);
        }
    };
    if (c.h >= 20 && c.h < 28 && s >= 0) { flag_wait(c, s, true, fl); out_duty<V_MEM>(c, s, c.h + 12, ldx); }
    else {
        ldx();
        if (c.h >= 28 && s >= -2 && s + 8 < NSEG) prefetch_x(c, s + 8, V_DUMP);
    }
}

DI void mem_attn(Ctx& c, int s) {
    const int l = lane_id(), g = l >> 4, cc = l & 15;
    const TB il = mktb(L_IMG + 12288 + g * 256 + rowp(l)), fl = mktb(L_F + g * 256 + (rowp(l) & ~16u));
    const u32 po = mpb(c, s), o1 = po + MP_PX + io((c.h >> 1) * 1024) + (c.h & 1) * 128, o2 = po + MP_PM + c.h * 128;
    const u32 v1 = opq(g * 256 + (cc & 7) * 16 + (cc & 8)), v2 = opq(cc < 8 ? cc * 16 + (g & 1) * 8 + (g >> 1) * 4 : 0x80000000u);
    v4u k[6][2], q[6], m[6];
    constexpr int PF = 3;
#pragma unroll
    for (int ks = 0; ks < PF; ++ks) { k[ks][0] = trfrag(il, ks * 1024); k[ks][1] = trfrag(il, 6144 + ks * 1024); }
    asm volatile("" :: "s"(o1), "s"(o2));
    bar();
    TS(c, 2);
    q[0] = trfrag(fl, 0); q[1] = trfrag(fl, 1024); m[0] = trfrag(fl, 16);
    SB();
    f4 t[3] = {{0, 0, 0, 0}, {0, 0, 0, 0}, {0, 0, 0, 0}};
#pragma unroll
    for (int ks = 0; ks < 6; ++ks) {
        t[1] = mfma(k[ks][0], q[ks], t[1]);
        SB();
        if (ks < 5) m[ks + 1] = trfrag(fl, (ks + 1) * 1024 + 16);
        SB();
        t[2] = mfma(k[ks][1], q[ks], t[2]);
        SB();
        if (ks + PF < 6) { k[ks + PF][0] = trfrag(il, (ks + PF) * 1024); k[ks + PF][1] = trfrag(il, 6144 + (ks + PF) * 1024); }
        SB();
        t[0] = mfma(m[ks], q[ks], t[0]);
        SB();
        if (ks < 4) q[ks + 2] = trfrag(fl, (ks + 2) * 1024);
        SB();
    }
    const float sc = 0.07216878364870322f * 1.4426950408889634f;
    float v[5];
#pragma unroll
    for (int i = 0; i < 4; ++i) v[i] = cc < 8 ? t[1][i] : t[2][i];
    v[4] = g < 2 ? (cc < 8 ? t[0][0] : t[0][1]) : (cc < 8 ? t[0][2] : t[0][3]);
    float mx = fmaxf(fmaxf(fmaxf(v[0], v[1]), fmaxf(v[2], v[3])), v[4]);
    mx = xmax(fmaxf(mx, ror8(mx))) * sc;
#pragma unroll
    for (int i = 0; i < 5; ++i) v[i] = __builtin_amdgcn_exp2f(__builtin_fmaf(v[i], sc, -mx));
    float sum = (v[0] + v[1]) + (v[2] + v[3]) + v[4];
    sum = xsum(sum + ror8(sum));
    const float inv = __builtin_amdgcn_rcpf(sum);
    gst64(c, o1, v1, v2u{pkc(v[0] * inv, v[1] * inv), pkc(v[2] * inv, v[3] * inv)});
    const float w = v[4] * inv;
    gst32(c, o2, v2, pkc(w, ror8(w)));
    __builtin_amdgcn_s_setprio(0);
}

DI void x_attn(Ctx& c, int s) {
    const int l = lane_id(), g = l >> 4, cc = l & 15;
    const TB il = mktb(L_IMG + g * 256 + rowp(l));
    f4 t[3] = {{0, 0, 0, 0}, {0, 0, 0, 0}, {0, 0, 0, 0}};
    const TB bl = {il.a + (c.wave - 1) * 6144, il.b + (c.wave - 1) * 6144};
    v4u k[7][4];
    k[0][0] = trfrag(bl, 0); k[0][1] = trfrag(il, L_F - L_IMG); k[0][2] = trfrag(il, 12288); k[0][3] = trfrag(il, 18432);
#pragma unroll
    for (int ks = 0; ks < 6; ++ks) {
        if (ks < 5) { const u32 o = (ks + 1) * 1024; k[ks + 1][0] = trfrag(bl, o); k[ks + 1][1] = trfrag(il, L_F - L_IMG + o); k[ks + 1][2] = trfrag(il, 12288 + o); k[ks + 1][3] = trfrag(il, 18432 + o); }
        SB();
        t[0] = mfma(k[ks][1], k[ks][0], t[0]); t[1] = mfma(k[ks][2], k[ks][0], t[1]); t[2] = mfma(k[ks][3], k[ks][0], t[2]);
        SB();
    }
    const float sc = 0.07216878364870322f * 1.4426950408889634f, ninf = __uint_as_float(opq(0xFF800000u));
    const int lim = (c.wave - 1) * 16 + cc - 4 * g;
    float mx = ninf;
#pragma unroll
    for (int k = 0; k < 3; ++k)
#pragma unroll
        for (int i = 0; i < 4; ++i) {
            const bool vis = k == 0 ? g >= 2 : (16 * (k - 1) + i <= lim);
            t[k][i] = vis ? t[k][i] : ninf; mx = fmaxf(mx, t[k][i]);
        }
    mx = xmax(mx) * sc;
    float sum = 0;
#pragma unroll
    for (int k = 0; k < 3; ++k)
#pragma unroll
        for (int i = 0; i < 4; ++i) { t[k][i] = __builtin_amdgcn_exp2f(__builtin_fmaf(t[k][i], sc, -mx)); sum += t[k][i]; }
    sum = xsum(sum);
    const float inv = __builtin_amdgcn_rcpf(sum);
    const v4u px = {pk(t[1][0] * inv, t[1][1] * inv), pk(t[1][2] * inv, t[1][3] * inv), pk(t[2][0] * inv, t[2][1] * inv), pk(t[2][2] * inv, t[2][3] * inv)};
    const v2u pm = {pk(t[0][0] * inv, t[0][1] * inv), pk(t[0][2] * inv, t[0][3] * inv)};
    const u32 po = pxb(c, s);
    gst(c, po + io((c.h * 2 + c.wave - 1) * 1024), l * 16, px);
    if (g >= 2) gst64(c, po + 131072 + io(((c.h >> 2) * 2 + c.wave - 1) * 1024) + (c.h & 3) * 256, cc * 16 + (g & 1) * 8, pm);
}

DI void qk_main(Ctx& c, const u16* wq, const u16* wk) {
    v8u W[24];
    load_x<2, 4>(c, c.p, 12 + (c.wave >> 1), c.wave & 1, L_X);
    load_weights<false>(c, c.wave < 4 ? wq : wk, W);
    if (c.par == 0 && c.p == 1 && c.h < NPAGE) { if (c.wave == 0) probe_page(c, c.h); bar(); }
    load_x<3, 4>(c, c.p, c.wave >> 1, c.wave & 1, L_X);
    barv();
    for (int s = c.p - 2; s < NSEG; s += 2) {
        asm volatile("" : "+s"(c.wave), "+s"(c.h), "+s"(c.par));
        if (s >= 0) {
            const int l = lane_id(), g = l >> 4, cc = l & 15;
            const u32 fa = L_F + ((c.wave & 3) * 48 + (cc ^ (4 * (cc >> 3)))) * 32 + (c.wave >> 2) * 16 + (g & 1) * 8;
            if (c.wave == 0) __builtin_amdgcn_s_setprio(3);
            crit_gemm1<L_MEM>(c, s, W, l, fa + (g >> 1) * 512, fa + 1024 + (g >> 1) * 4);
            if (c.wave == 0) mem_attn(c, s);
            else {
                bar();
                TS(c, 2);
                if (c.wave < 3) x_attn(c, s);
                else {
                    if (s + 2 < NSEG) { load_x<1, 1>(c, s + 2, 9 + c.wave, 0, L_X); load_x<1, 1>(c, s + 2, 9 + c.wave, 1, L_X); }
                    if (c.wave == 4) __builtin_amdgcn_s_sleep(QSLEEP);
                }
            }
            TS(c, 3);
            if (s < 2) {
                const v4u sv = sentv();
                for (int i = c.wave * 64 + lane_id(); i < 30 * 40; i += 512) {
                    const int st = c.p + 4 + 2 * (i / 40), e = i % 40;
                    const u32 o = e < 32 ? MP_PX + io((c.h >> 1) * 1024) + ((e >> 3) * 16 + (c.h & 1) * 8 + (e & 7)) * 16 : MP_PM + c.h * 128 + (e & 7) * 16;
                    gst(c, 0, mpb(c, st) + o, sv);
                }
            }
        }
        u32 fl = SENT;
        auto mid = [&](int k) {
            TS(c, 5 + k);
            if (k == 2) { fl = flag_load(c, s, false); return; }
            barv();
            if (k == 0 && s >= 0) {
                if (c.wave == 7) gst32(c, fb(c, s) + 512 + c.h * 4, 0, opq(1u));
                else if (c.wave < 6 && s + 2 < NSEG) load_x<1, 1>(c, s + 2, 17 + (c.wave >> 1), c.wave & 1, L_X);
            }
        };
        if (s + 2 < NSEG) {
            bulk_gemm1(c, W, mid);
            bar();
        } else mid(0);
        TS(c, 4);
        fl = pending(fl);
        if (s < 0) { load_x<3, 4>(c, s + 4, c.wave >> 1, c.wave & 1, L_X); get_mask(c); }
        else { flag_wait(c, s, false, fl); out_duty<L_MEM>(c, s, c.h, [&] { if (s + 4 < NSEG) load_x<3, 4>(c, s + 4, c.wave >> 1, c.wave & 1, L_X); }); }
        TSF(c, s);
    }
}

template <class F> DI void bulk_v(Ctx& c, const v8u (&W)[24], int s, F mid) {
    const int l = lane_id();
    const TB wl = wbase<false>(c, l);
    const u32 ab = opq(V_X + xfrag(l));
    f4 acc[2][3];
#pragma unroll
    for (int m = 0; m < 2; ++m)
#pragma unroll
        for (int u = 0; u < 3; ++u) acc[m][u] = f4{0, 0, 0, 0};
    v4u a[22][2], b[5][3];
    a[0][0] = lds128(ab); a[0][1] = lds128(ab + 1024);
#pragma unroll
    for (int u = 0; u < 3; ++u) b[0][u] = trfrag(wl, u * 256);
    a[1][0] = lds128(ab + 2048); a[1][1] = lds128(ab + 3072);
#pragma unroll
    for (int q = 0; q < 20; ++q) {
        if (q == 8) {
            const int l2 = lane_id();
            const unsigned char* g = (const unsigned char*)c.x + (size_t)s * 40960 + 960;
            const u32 vo = (l2 & 15) * 1280 + (l2 >> 4) * 16;
            a[19][0] = *(const v4u*)(g + vo); a[19][1] = *(const v4u*)(g + 20480 + vo);
        }
        if (q == 13) mid();
        SB();
        if (q >= 4) {
            const int ks = q - 4;
#pragma unroll
            for (int m = 0; m < 2; ++m) {
#pragma unroll
                for (int u = 0; u < 3; ++u) acc[m][u] = mfma(a[q][m], (ks & 1) ? hi4(W[u * 8 + (ks >> 1)]) : lo4(W[u * 8 + (ks >> 1)]), acc[m][u]);
                SB();
                if (q < 17) a[q + 2][m] = lds128(ab + (q + 2) * 2048 + m * 1024);
                SB();
            }
        } else {
#pragma unroll
            for (int u = 0; u < 3; ++u) {
                acc[0][u] = mfma(a[q][0], b[q][u], acc[0][u]); acc[1][u] = mfma(a[q][1], b[q][u], acc[1][u]);
                if (q < 3) { SB(); b[q + 1][u] = trfrag(wl, (q + 1) * 24576 + u * 256); SB(); }
            }
            a[q + 2][0] = lds128(ab + (q + 2) * 2048); a[q + 2][1] = lds128(ab + (q + 2) * 2048 + 1024);
            SB();
        }
    }
    const int l3 = lane_id(), cc = l3 & 15;
    const u32 ia = L_IMG + (c.wave * 48 + (cc ^ (4 * (cc >> 3)))) * 32 + (l3 >> 4) * 8;
    v2u v[2][3];
#pragma unroll
    for (int m = 0; m < 2; ++m)
#pragma unroll
        for (int u = 0; u < 3; ++u) v[m][u] = v2u{pk(acc[m][u][0], acc[m][u][1]), pk(acc[m][u][2], acc[m][u][3])};
    TS(c, 3);
    bar();
#pragma unroll
    for (int m = 0; m < 2; ++m)
#pragma unroll
        for (int u = 0; u < 3; ++u) st64(ia + m * 6144 + u * 512, v[m][u]);
}

DI void v_main(Ctx& c, const u16* wv) {
    v8u W[24];
    load_weights<false>(c, wv, W);
    for (int s = c.p - 4; s < NSEG; s += 2) {
        asm volatile("" : "+s"(c.wave), "+s"(c.h), "+s"(c.par));
        if (s >= 0) {
            const int l = lane_id(), g = l >> 4, cc = l & 15;
            const int rho = ((cc >> 2) << 1) | (cc & 1);
            const u32 a = V_VS + ((cc >> 1) & 1) * 16 + (g & 1) * 8;
            const int t0 = c.wave * 3 + (g >> 1), t2 = c.wave * 3 + 2;
            crit_gemm1<V_MEM>(c, s, W, l, a + (t0 * 8 + (rho ^ (4 * (t0 & 1)))) * 32, a + (t2 * 8 + (rho ^ (4 * (t2 & 1)))) * 32 + (g >> 1) * 4);
            bar();
            TS(c, 2);
            if (c.h >= 20) __builtin_amdgcn_s_sleep(VSLEEP);
            else if (s < NSEG - 1) { flag_wait(c, s, true, 1); node_duty(c, s, c.h); }
            if (c.h < 20 && s < 2) {
                const v4u sv = sentv();
                for (int i = c.wave * 64 + lane_id(); i < 30 * 32; i += 256) {
                    const int st = 5 + c.p + 2 * (i >> 5);
                    if (st < NSEG) gst(c, 0, mpb(c, st) + io(c.h * 512) + (i & 31) * 16, sv);
                }
            }
        } else {
            barv();
            if (s >= -2) get_mask(c);
        }
        u32 fl = SENT;
        if (s + 4 < NSEG) bulk_v(c, W, s + 4, [&] { fl = flag_load(c, s, true); });
        else bar();
        TS(c, 4);
        vm_duty(c, s, pending(fl));
        TSF(c, s);
    }
}

DI u32 wo_off(int i, int ks) { return ((i - 8) * 2 + ks / 3) * 24576 + (ks % 3) * 4096; }
DI void bulk_gemm2(const Ctx& c, const v8u (&W)[24], int s) {
    const int l = lane_id();
    const TB wl = wbase<true>(c, l), il = mktb(L_IMG + (l >> 4) * 256 + rowp(l));
    const u32 uo = ub(c, s, c.wave & 3) + io(c.h * 1024);
    v4u a[20][2], b[20];
#pragma unroll
    for (int i = 0; i < 4; ++i) a[i >> 1][i & 1] = trfrag(il, (i & 1) * 6144 + (i >> 1) * 1024);
#pragma unroll
    for (int ig = 0; ig < 3; ++ig) {
        const int nt = ig == 0 ? 4 : 3;
        f4 acc[2][4];
#pragma unroll
        for (int j = 0; j < 4; ++j) { acc[0][j] = f4{0, 0, 0, 0}; acc[1][j] = f4{0, 0, 0, 0}; }
#pragma unroll
        for (int ks = 0; ks < 6; ++ks) {
            const int st = ig * 6 + ks;
#pragma unroll
            for (int m = 0; m < 2; ++m) {
                SB();
#pragma unroll
                for (int j = 0; j < nt; ++j) {
                    const int i = ig == 0 ? j : (j < 2 ? 2 + 2 * ig + j : 7 + ig);
                    acc[m][j] = mfma(a[st][m], i < 8 ? ((ks & 1) ? hi4(W[i * 3 + (ks >> 1)]) : lo4(W[i * 3 + (ks >> 1)])) : b[st], acc[m][j]);
                    if (m == 0 && j == 0 && st >= 5 && st < 17) { SB(); b[st + 1] = trfrag(wl, wo_off(7 + (st + 1) / 6, (st + 1) % 6)); SB(); }
                }
                SB();
                if (st < 16) a[st + 2][m] = trfrag(il, m * 6144 + ((st + 2) % 6) * 1024);
            }
        }
        SB();
#pragma unroll
        for (int j = 0; j < nt; ++j) {
            const int i = ig == 0 ? j : (j < 2 ? 2 + 2 * ig + j : 7 + ig);
            const v4u v = {pk(acc[0][j][0], acc[0][j][1]), pk(acc[0][j][2], acc[0][j][3]), pk(acc[1][j][0], acc[1][j][1]), pk(acc[1][j][2], acc[1][j][3])};
            gst(c, uo + wo_col(i) * 65536, l * 16, v);
        }
        SB();
    }
}

DI void o_main(Ctx& c, const u16* wo) {
    v8u W[24];
    load_weights<true>(c, wo, W);
    load_x<2, 2>(c, c.p, 16 + ((c.wave >> 1) & 1), c.wave & 1, V_XH);
    load_x<4, 2>(c, c.p, (c.wave >> 1) & 1, c.wave & 1, V_XL);
    for (int s = c.p - 4; s < NSEG; s += 2) {
        asm volatile("" : "+s"(c.wave), "+s"(c.h), "+s"(c.par));
        if (s >= 0) {
            const int l = lane_id(), g = l >> 4;
            const TB wl = wbase<true>(c, l), il = mktb(V_VS + g * 256 + rowp(l));
            const u32 idx = 0x4444u + (l & 8) * 0x1555u + ((l & 8) >> 2);
            const u32 za = ((g >> 1) + 2 * (g & 1)) * 65536 + (l & 15) * 16;
            const u32 zo = zb(c, s) + (c.wave & 3) * 16384 + c.h * 256;
            stage_mem<V_MEM>(c, s);
            if (c.h < 20) flag_wait(c, s, true, 1);
            v8u T[3];
#pragma unroll
            for (int f = 0; f < 3; ++f) T[f] = cat8(trfrag(wl, wo_off(8, 2 * f)), trfrag(wl, wo_off(8, 2 * f + 1)));
            __builtin_amdgcn_s_setprio(3);
            bar();
            TS(c, 2);
            v4u a[3];
#pragma unroll
            for (int f = 0; f < 3; ++f) a[f] = trfrag(il, f * 1024);
            SB();
            f4 acc[6][2];
            u32 xp[5][2];
#pragma unroll
            for (int pr = 0; pr < 6; ++pr) {
                if (pr < 5) {
                    acc[pr][0] = f4{0, 0, 0, 0}; acc[pr][1] = f4{0, 0, 0, 0};
#pragma unroll
                    for (int f = 0; f < 3; ++f)
#pragma unroll
                        for (int j = 0; j < 2; ++j) {
                            const int o = 2 * pr + j, i = o == 0 ? 8 : (o == 4 ? 9 : (o < 4 ? o - 1 : o - 2));
                            acc[pr][j] = smf(a[f], i < 8 ? W[i * 3 + f] : T[f], acc[pr][j], idx);
                        }
                }
                if (pr == 0) {
                    SB();
#pragma unroll
                    for (int f = 0; f < 3; ++f) T[f] = cat8(trfrag(wl, wo_off(9, 2 * f)), trfrag(wl, wo_off(9, 2 * f + 1)));
                } else {
                    const int k = pr - 1;
                    float x[4];
#pragma unroll
                    for (int i = 0; i < 4; ++i) x[i] = fold32(acc[k][0][i], acc[k][1][i]);
                    xp[k][0] = pkc(x[0], x[1]); xp[k][1] = pkc(x[2], x[3]);
                    if (k == 1 || k == 3 || k == 4) {
                        const int k0 = k == 4 ? 4 : k - 1;
                        auto s0 = __builtin_amdgcn_permlane16_swap(xp[k0][0], xp[k][0], false, false);
                        auto s1 = __builtin_amdgcn_permlane16_swap(xp[k0][1], xp[k][1], false, false);
                        const v4u v = {s0[0], s1[0], s0[1], s1[1]};
                        if (k < 4 || !(g & 1)) gst(c, zo + (k >> 1) * 262144, za, v);
                    }
                }
                SB();
            }
            __builtin_amdgcn_s_setprio(0);
            TS(c, 3);
            if (c.h < 20 && s < NSEG - 1) node_duty(c, s, c.h);
        } else {
            barv();
            if (s >= -2) get_mask(c);
        }
        const bool bulk = s >= -2 && s + 2 < NSEG;
        if (bulk) {
            __builtin_amdgcn_s_setprio(UPRIO);
            bulk_gemm2(c, W, s + 2);
            __builtin_amdgcn_s_setprio(0);
            const int l = lane_id(), g = l >> 4;
            const v4u sv = sentv();
            const u32 za = ((g >> 1) + 2 * (g & 1)) * 65536 + (l & 15) * 16;
#pragma unroll
            for (int k = 0; k < 3; ++k)
                if (k < 2 || !(g & 1)) gst(c, zb(c, s + 2) + k * 262144 + (c.wave & 3) * 16384 + c.h * 256, za, sv);
        }
        u32 fl = flag_load(c, s, true);
        TS(c, 0);
        bar();
        TS(c, 1);
        fl = pending(fl);
        if (bulk) { asm volatile("s_waitcnt vmcnt(0)" ::: "memory"); gst32(c, fb(c, s + 2) + (c.wave & 3) * 128 + c.h * 4, 0, opq(1u)); }
        TS(c, 4);
        vm_duty(c, s, fl);
        TSF(c, s);
    }
}

__global__ void __launch_bounds__(512, 2) memtr_kernel(Args a) {
    Ctx c;
    const int wg = blockIdx.x;
    c.wave = __builtin_amdgcn_readfirstlane(threadIdx.x >> 6);
    const int br = (wg & 7) >> 2;
    const int idx = (wg >> 3) * 4 + (wg & 3);
    c.h = idx & 31; c.p = idx >> 6;
    c.r = __builtin_amdgcn_make_buffer_rsrc(a.ws, 0, WS_SPAN, 0x00020000);
    c.par = br ? ~0u : 0u;
    c.spin = 0;
    c.x = br ? a.x[1] : a.x[0]; c.mem0 = br ? a.mem0[1] : a.mem0[0]; c.out = a.out + br * 640;
    if (((idx >> 5) & 1) == 0) qk_main(c, br ? a.w[1][0] : a.w[0][0], br ? a.w[1][1] : a.w[0][1]);
    else {
        if (c.h >= 28) { prefetch_x(c, c.p + 2, V_XL + 7 * 2048); prefetch_x(c, c.p + 4, V_XL + 7 * 2048); }
        if (c.wave < 7) { load_x<1, 1>(c, c.p, 8 + c.wave, 0, V_XL); load_x<1, 1>(c, c.p, 8 + c.wave, 1, V_XL); }
        if (c.wave < 4) v_main(c, br ? a.w[1][2] : a.w[0][2]);
        else o_main(c, br ? a.w[1][3] : a.w[0][3]);
    }
}

extern "C" void kernel_launch(void* const* d_in, const int* in_sizes, int n_in, void* d_out, int out_size, void* d_ws, size_t ws_size, hipStream_t stream) {
    Args a;
    for (int b = 0; b < 2; ++b) {
        a.x[b] = (const u16*)d_in[7 * b]; a.mem0[b] = (const u16*)d_in[7 * b + 1];
        for (int k = 0; k < 4; ++k) a.w[b][k] = (const u16*)d_in[7 * b + 2 + k];
    }
    a.out = (u16*)d_out; a.ws = (unsigned char*)d_ws;
    (void)hipMemsetAsync((unsigned char*)d_ws + INIT_OFF, 0xFF, INIT_BYTES, stream);
    memtr_kernel<<<256, 512, 0, stream>>>(a);
}
```

```cpp
#include <hip/hip_runtime.h>
#include <stdint.h>

typedef unsigned u32;
typedef unsigned short u16;
typedef u32 v2u __attribute__((ext_vector_type(2)));
typedef u32 v4u __attribute__((ext_vector_type(4)));
typedef u32 v8u __attribute__((ext_vector_type(8)));
typedef float f4 __attribute__((ext_vector_type(4)));
typedef float f2 __attribute__((ext_vector_type(2)));
typedef short s4 __attribute__((ext_vector_type(4)));
typedef __bf16 b2 __attribute__((ext_vector_type(2)));
typedef __bf16 b8 __attribute__((ext_vector_type(8)));
typedef __bf16 b16 __attribute__((ext_vector_type(16)));
#define AS3 __attribute__((address_space(3)))
#define DI __device__ __forceinline__

constexpr int NSEG = 64;
constexpr u32 SENT = 0xFFFFFFFFu;
constexpr int NPAGE = 17;
constexpr u32 WS_SPAN = NPAGE * 2097152;
constexpr u32 INIT_OFF = 2097152 - 147456, INIT_BYTES = 147456 + 5 * 65536;
constexpr u32 MP_PM = 20480, MP_PX = 32768;
constexpr u32 L_IMG = 98304, L_X = 122880, L_MEM = L_X + 24576, L_F = L_X + 34816;
constexpr u32 V_MEM = L_IMG + 12288, V_VS = V_MEM + 10240, V_DUMP = V_VS + 3072, V_X = V_DUMP + 1024;
#define QSLEEP 5
#define UPRIO 2
#define QM0 7
#define QM1 1
#define NSLEEP 5
#define OSLEEP 0
#define OVSLEEP 0
#define UPRE 6
#define VPRE 4
#define VPRIO 3
#define OPRIO 3
#define PSLEEP 3
#ifndef NVX
#define NVX 3
#endif
#define VSLEEP 0
constexpr u32 SPIN_MAX = 1u << 20;

struct Args { const u16* x[2]; const u16* mem0[2]; const u16* w[2][4]; u16* out; unsigned char* ws; };

__shared__ __attribute__((aligned(1024))) unsigned char lds[163840];

DI v4u lds128(u32 a) { return *(AS3 v4u*)((AS3 unsigned char*)lds + a); }
DI f2 lds64f(u32 a) { return *(AS3 f2*)((AS3 unsigned char*)lds + a); }
DI void st128(u32 a, v4u v) { *(AS3 v4u*)((AS3 unsigned char*)lds + a) = v; }
DI void st64(u32 a, v2u v) { *(AS3 v2u*)((AS3 unsigned char*)lds + a) = v; }
DI void st32(u32 a, u32 v) { *(AS3 u32*)((AS3 unsigned char*)lds + a) = v; }
DI v2u ldtr(u32 a) { return __builtin_bit_cast(v2u, __builtin_amdgcn_ds_read_tr16_b64_v4i16((AS3 s4*)((AS3 unsigned char*)lds + a))); }
struct TB { u32 a, b; };
DI u32 opq(u32 v);
DI TB mktb(u32 a) { TB t = {opq(a), opq(a ^ 128u)}; return t; }
DI v4u trfrag(TB t, u32 off) { v2u lo = ldtr(t.a + off), hi = ldtr(t.b + off); v4u r = {lo.x, lo.y, hi.x, hi.y}; return r; }
DI int lane_id() { int l; asm volatile("v_mbcnt_lo_u32_b32 %0, -1, 0\n\tv_mbcnt_hi_u32_b32 %0, -1, %0" : "=v"(l)); return l; }
DI u32 opq(u32 v) { asm volatile("" : "+v"(v)); return v; }
DI float opqf(float v) { asm volatile("" : "+v"(v)); return v; }
DI v4u sentv() { const u32 v = opq(SENT); v4u r = {v, v, v, v}; return r; }
DI u32 rowp(int l) { return ((((l & 15) >> 2) ^ ((l >> 2) & 4)) * 32 + (l & 3) * 8); }
DI v8u cat8(v4u a, v4u b) { return __builtin_shufflevector(a, b, 0, 1, 2, 3, 4, 5, 6, 7); }
DI v4u lo4(v8u a) { return __builtin_shufflevector(a, a, 0, 1, 2, 3); }
DI v4u hi4(v8u a) { return __builtin_shufflevector(a, a, 4, 5, 6, 7); }
DI f4 mfma(v4u a, v4u b, f4 c) { return __builtin_amdgcn_mfma_f32_16x16x32_bf16(__builtin_bit_cast(b8, a), __builtin_bit_cast(b8, b), c, 0, 0, 0); }
DI f4 smf(v4u a, v8u b, f4 c, u32 idx) { return __builtin_amdgcn_smfmac_f32_16x16x64_bf16(__builtin_bit_cast(b8, a), __builtin_bit_cast(b16, b), c, (int)idx, 0, 0); }
DI u32 pk(float a, float b) { b2 r = {(__bf16)a, (__bf16)b}; return __builtin_bit_cast(u32, r); }
DI u32 pkc(float a, float b) { u32 r = pk(a, b); return r < 0xFFFFFFFEu ? r : 0xFFFFFFFEu; }
DI void bar() { asm volatile("s_waitcnt lgkmcnt(0)\n\ts_barrier" ::: "memory"); }
DI void barv() { asm volatile("s_waitcnt vmcnt(0) lgkmcnt(0)\n\ts_barrier" ::: "memory"); }
template <bool LOW, bool VMEM> DI void bar_half(int wave) {
    if (VMEM) asm volatile("s_cmp_lt_u32 %0, 4\n\ts_cbranch_scc%1 LBH%=\n\ts_waitcnt vmcnt(0) lgkmcnt(0)\n\ts_barrier\nLBH%=:" :: "s"(wave), "n"(LOW ? 0 : 1) : "memory", "scc");
    else asm volatile("s_cmp_lt_u32 %0, 4\n\ts_cbranch_scc%1 LBH%=\n\ts_waitcnt lgkmcnt(0)\n\ts_barrier\nLBH%=:" :: "s"(wave), "n"(LOW ? 0 : 1) : "memory", "scc");
}
#define WHINT " nt"
#define XHINT ""
#define HALFSW 0
DI void dma(const void* sbase, u32 voff, u32 ldst) {
    asm volatile("s_mov_b32 m0, %2\n\ts_nop 0\n\tglobal_load_lds_dwordx4 %0, %1" WHINT :: "v"(voff), "s"(sbase), "s"(ldst) : "memory");
}
template <int GO, int LO> DI void dmai(const void* sbase, u32 voff, u32 ldst) {
    asm volatile("s_add_u32 m0, %2, %3\n\ts_nop 0\n\tglobal_load_lds_dwordx4 %0, %1 offset:%4" XHINT :: "v"(voff), "s"(sbase), "s"(ldst), "n"(LO - GO), "n"(GO) : "memory", "scc");
}
DI float fold32(float a, float b) { auto s = __builtin_amdgcn_permlane32_swap(__float_as_uint(a), __float_as_uint(b), false, false); return __uint_as_float(s[0]) + __uint_as_float(s[1]); }
DI float xmax(float x) {
    auto s = __builtin_amdgcn_permlane16_swap(__float_as_uint(x), __float_as_uint(x), false, false); x = fmaxf(__uint_as_float(s[0]), __uint_as_float(s[1]));
    auto t = __builtin_amdgcn_permlane32_swap(__float_as_uint(x), __float_as_uint(x), false, false); return fmaxf(__uint_as_float(t[0]), __uint_as_float(t[1]));
}
DI float xsum(float x) {
    auto s = __builtin_amdgcn_permlane16_swap(__float_as_uint(x), __float_as_uint(x), false, false); x = __uint_as_float(s[0]) + __uint_as_float(s[1]);
    auto t = __builtin_amdgcn_permlane32_swap(__float_as_uint(x), __float_as_uint(x), false, false); return __uint_as_float(t[0]) + __uint_as_float(t[1]);
}
DI float ror8(float x) { return __uint_as_float(__builtin_amdgcn_update_dpp(0, __float_as_uint(x), 0x128, 0xF, 0xF, true)); }
DI u32 umax(u32 a, u32 b) { return a > b ? a : b; }
DI u32 max4(v4u v) { return umax(umax(v.x, v.y), umax(v.z, v.w)); }

struct Ctx {
    __amdgpu_buffer_rsrc_t r; u32 par; int h, p, wave; u32 spin;
    const u16* x; const u16* mem0; u16* out;
};
DI constexpr u32 io(u32 o) { return ((o >> 13) << 14) | (o & 8191u); }
DI u32 pgb(const Ctx& c, u32 page) { return (page << 21) | (((c.par >> page) & 1u) << 13); }
DI u32 mpb(const Ctx& c, int s) { return pgb(c, 1 + ((u32)s >> 5)) + ((u32)s & 31u) * 65536u; }
DI u32 zb(const Ctx& c, int s) { const u32 b = (u32)s & 7u, q = (b * 11u) >> 5; return pgb(c, 3 + q) + (b - 3u * q) * 655360u; }
DI u32 ub(const Ctx& c, int s, u32 w) { const u32 b = ((u32)s & 7u) * 4u + w, q = (b * 43u) >> 7; return pgb(c, 6 + q) + (b - 3u * q) * 655360u; }
DI u32 pxb(const Ctx& c, int s) { return pgb(c, 0) + ((u32)s % 6u) * 163840u; }
DI u32 fb(const Ctx& c, int s) { return pgb(c, 0) + (INIT_OFF + 16384) + io((u32)s * 1024u); }
DI v4u gld(const Ctx& c, u32 off, u32 voff) { return __builtin_amdgcn_raw_buffer_load_b128(c.r, voff, off, 16); }
DI void gst(const Ctx& c, u32 off, u32 voff, v4u v) { __builtin_amdgcn_raw_buffer_store_b128(v, c.r, voff, off, 16); }
DI void gst64(const Ctx& c, u32 off, u32 voff, v2u v) { __builtin_amdgcn_raw_buffer_store_b64(v, c.r, voff, off, 16); }
DI void gst32(const Ctx& c, u32 off, u32 voff, u32 v) { __builtin_amdgcn_raw_buffer_store_b32(v, c.r, voff, off, 16); }

#define TS(c, k)
#define TSF(c, s)
#define TS1(c, k) TS(c, k)
#define TS2(c, k)
#define SB() __builtin_amdgcn_sched_barrier(0)

template <int N> DI void poll(Ctx& c, const u32 (&off)[N], const u32 (&voff)[N], v4u (&v)[N]) {
    for (;;) {
        asm volatile("" ::: "memory");
        u32 m = 0;
#pragma unroll
        for (int k = 0; k < N; ++k) v[k] = gld(c, off[k], voff[k]);
#pragma unroll
        for (int k = 0; k < N; ++k) m = umax(m, max4(v[k]));
        if (__builtin_amdgcn_ballot_w64(m == SENT) == 0) return;
        if (++c.spin > SPIN_MAX) return;
    }
}

DI void probe_page(const Ctx& c, int pg) {
    u32 best[2] = {~0u, ~0u};
    const u32 o = pg * 2097152 + 1048576;
    for (int r = 0; r < 3; ++r) {
        __builtin_amdgcn_raw_buffer_store_b32(opq(SENT), c.r, 0, o, 16);
        __builtin_amdgcn_raw_buffer_store_b32(opq(SENT), c.r, 0, o + 8192, 16);
        asm volatile("s_waitcnt vmcnt(0)" ::: "memory");
#pragma unroll
        for (int k = 0; k < 2; ++k) {
            const u32 t0 = (u32)__builtin_amdgcn_s_memtime();
            const u32 v = __builtin_amdgcn_raw_buffer_load_b32(c.r, 0, o + k * 8192, 16);
            asm volatile("s_waitcnt vmcnt(0)" :: "v"(v) : "memory");
            const u32 d = (u32)__builtin_amdgcn_s_memtime() - t0;
            best[k] = d < best[k] ? d : best[k];
        }
    }
    __builtin_amdgcn_raw_buffer_store_b32(opq(best[1] < best[0] ? 1u : 0u), c.r, 0, INIT_OFF + pg * 4, 16);
}
DI void get_mask(Ctx& c) {
    const int l = lane_id();
    const u32 vo = (l < NPAGE ? l : 0) * 4;
    for (;;) {
        asm volatile("" ::: "memory");
        const u32 v = __builtin_amdgcn_raw_buffer_load_b32(c.r, vo, INIT_OFF, 16);
        if (__builtin_amdgcn_ballot_w64(v == SENT) == 0 || ++c.spin > SPIN_MAX) { c.par ^= (u32)__builtin_amdgcn_ballot_w64(v == 1u); return; }
    }
}

template <int N, int ST, int I = 0> DI void dma_x(const unsigned char* b, u32 voff, u32 dst) {
    if constexpr (I < N) { dmai<I * ST * 64, I * ST * 2048>(b, voff, dst); dma_x<N, ST, I + 1>(b, voff, dst); }
}
template <int N, int ST> DI void load_x(const Ctx& c, int s, int ks, int m, u32 dst) {
    const int l = lane_id();
    dma_x<N, ST>((const unsigned char*)c.x + (s * 40960 + m * 20480 + ks * 64), (l >> 2) * 1280 + (((l & 3) ^ (-(l >> 4) & 3)) << 4), dst + ks * 2048 + m * 1024);
}
DI u32 xfrag(int l) { return (l & 15) * 64 + (((l >> 4) ^ (-(l >> 2) & 3)) << 4); }
constexpr u32 V_XH = V_X - 16 * 2048, V_XL = V_X + 4 * 2048, L_XH = L_X - 16 * 2048, L_XL = L_X + 4 * 2048;
DI void prefetch_x(const Ctx& c, int s, u32 dump) {
    const unsigned char* b = (const unsigned char*)c.x + (s * 40960 + c.wave * 5120);
    const u32 voff = lane_id() * 16;
    dmai<0, 0>(b, voff, dump); dmai<1024, 0>(b, voff, dump); dmai<2048, 0>(b, voff, dump); dmai<3072, 0>(b, voff, dump); dmai<0, 0>(b + 4096, voff, dump);
}
DI u32 flag_load(const Ctx& c, int s, bool vm) {
    const int l = lane_id();
    const bool node = vm && c.h < 20;
    const u32 w = node ? (c.h & 1) * 2 : (c.h & 3), d = node ? 128 : 512 - w * 128;
    return __builtin_amdgcn_raw_buffer_load_b32(c.r, (l & 3) * 4 + ((l & 4) ? d : 0) + (node ? (l & 8) * 8 : 0), fb(c, s < 0 ? 0 : s) + w * 128 + (node ? c.wave & 3 : c.wave) * 16, 16);
}
DI u32 pending(u32 v) { u32 r = (u32)__builtin_amdgcn_ballot_w64(v == SENT); asm volatile("" : "+s"(r)); return r; }
DI void flag_wait(Ctx& c, int s, bool vm, u32 miss) {
    while (miss != 0 && ++c.spin < SPIN_MAX) { asm volatile("" ::: "memory"); miss = pending(flag_load(c, s, vm)); }
    asm volatile("" ::: "memory");
}

DI u32 mem_in(const Ctx& c, int s) { return mpb(c, s) + io(c.wave * 2048); }
template <u32 LM> DI void stage_mem(Ctx& c, int s, u32 o) {
    if (c.wave < 5) {
        const int l = lane_id();
        v4u v[2];
        if (s == 0) {
            const __amdgpu_buffer_rsrc_t mr = __builtin_amdgcn_make_buffer_rsrc((void*)c.mem0, 0, 10240, 0x00020000);
            const u32 m = (l & 7) * 1280 + (l >> 4) * 32 + (l & 8) / 2;
#pragma unroll
            for (int k = 0; k < 2; ++k)
#pragma unroll
                for (int j = 0; j < 4; ++j) v[k][j] = __builtin_amdgcn_raw_buffer_load_b32(mr, m + k * 128 + j * 8, c.wave * 256, 0);
        } else {
            const u32 off[2] = {o, o}; const u32 voff[2] = {(u32)l * 16, (u32)l * 16 + 1024};
            poll<2>(c, off, voff, v);
        }
        st128(LM + c.wave * 2048 + l * 16, v[0]);
        st128(LM + c.wave * 2048 + 1024 + l * 16, v[1]);
    }
    barv();
}

struct NodeA { u32 u0, u1, px, pm, z, mo, mi, zo; };
DI NodeA node_addr(const Ctx& c, int t) {
    const u32 n = c.h, uo = (n >> 1) * 65536 + io(c.wave * 4096), po = mpb(c, t);
    NodeA a = {ub(c, t, (2 * n) & 3) + uo, ub(c, t, (2 * n + 1) & 3) + uo, po + MP_PX + io(c.wave * 2048), po + MP_PM + c.wave * 512,
               zb(c, t) + n * 32768 + c.wave * 1024, mpb(c, t + 1) + io((2 * n + (c.wave & 1)) * 256),
               po + io(c.wave * 2048), zb(c, t) + (c.wave & 3) * 16384 + n * 256};
    asm volatile("" : "+s"(a.u0), "+s"(a.u1), "+s"(a.px), "+s"(a.pm), "+s"(a.z), "+s"(a.mo), "+s"(a.mi), "+s"(a.zo));
    return a;
}
template <int I0, int I1> DI void node_u(const Ctx& c, const NodeA& na, v4u (&u)[2][4]) {
    const u32 l16 = lane_id() * 16 + (c.h < 20 ? 0u : 0x80000000u);
#pragma unroll
    for (int i = I0; i < I1; ++i) u[i >> 2][i & 3] = gld(c, i >> 2 ? na.u1 : na.u0, l16 + (i & 3) * 1024);
}
template <int SLP> DI void node_duty(Ctx& c, const NodeA& na, v4u (&u)[2][4]) {
    const int l = lane_id();
    const u32 l16 = l * 16;
    const u32 mo = na.mo;
    const u32 off[3] = {na.px, na.px, na.pm}, zoff[2] = {na.z, na.z + 16384};
    const u32 voff[3] = {l16, l16 + 1024, (u32)((l >> 4) * 128 + (l & 7) * 16)}, zvoff[2] = {l16, l16};
    v4u v[3], zz[2];
    const int r16 = l >> 2, jj = l & 3;
    const u32 a = opq(V_MEM + (jj * 16 + c.wave * 8 + (r16 & 7)) * 16 + (r16 >> 3) * 8), ra = opq(V_MEM + c.wave * 1024 + l16);
    __builtin_amdgcn_s_sleep(SLP);
    TS(c, 5);
    zz[0] = gld(c, zoff[0], l16); zz[1] = gld(c, zoff[1], l16);
    __builtin_amdgcn_s_sleep(PSLEEP);
    poll<3>(c, off, voff, v);
    if (__builtin_amdgcn_ballot_w64(umax(max4(zz[0]), max4(zz[1])) == SENT) != 0) { ++c.spin; poll<2>(c, zoff, zvoff, zz); }
    TS(c, 6);
    f4 lo[2], hi[2];
#pragma unroll
    for (int j = 0; j < 2; ++j) {
        const f4 z = {0, 0, 0, 0};
        lo[j] = mfma(u[j][0], v[0], z); hi[j] = mfma(u[j][1], v[0], z);
        lo[j] = mfma(u[j][2], v[1], lo[j]); hi[j] = mfma(u[j][3], v[1], hi[j]);
        lo[j] = mfma(zz[j], v[2], lo[j]);
    }
    f4 r;
#pragma unroll
    for (int i = 0; i < 4; ++i) { float a = lo[0][i] + ror8(hi[0][i]), b = ror8(lo[1][i]) + hi[1][i]; r[i] = (l & 8) ? b : a; }
    st128(ra, __builtin_bit_cast(v4u, r));
    bar();
    if (c.wave < 2) {
        f2 s = lds64f(a);
#pragma unroll
        for (int w = 1; w < 8; ++w) s += lds64f(a + w * 1024);
        gst32(c, mo, l * 4, pkc(s.x, s.y));
    } else __builtin_amdgcn_s_sleep(3);
    TS(c, 7);
}

template <class F> DI void out_part(Ctx& c, int t, int j, f4 (&a)[2], F mid) {
    const u32 l16 = lane_id() * 16;
    a[0] = f4{0, 0, 0, 0}; a[1] = f4{0, 0, 0, 0};
    v4u u[4], px[4][2], z[3];
#pragma unroll
    for (int k = 0; k < 4; ++k) {
        u[k] = gld(c, ub(c, t, j & 3) + (j >> 2) * 65536 + io(c.wave * 4096), l16 + k * 1024);
        px[k][0] = gld(c, pxb(c, t) + c.wave * 16384 + (k >> 1) * 4096, l16 + (k & 1) * 2048);
        px[k][1] = gld(c, pxb(c, t) + c.wave * 16384 + (k >> 1) * 4096, l16 + (k & 1) * 2048 + 1024);
    }
    const u32 po = pxb(c, t) + 131072 + io(c.wave * 2048);
    const u32 off[1] = {zb(c, t) + j * 16384 + c.wave * 1024}; const u32 voff[1] = {l16};
    SB();
    mid();
    SB();
    a[0] = mfma(u[0], px[0][0], a[0]); a[1] = mfma(u[0], px[0][1], a[1]);
    SB();
    z[0] = gld(c, off[0], l16); z[1] = gld(c, po, l16); z[2] = gld(c, po, l16 + 1024);
    SB();
#pragma unroll
    for (int k = 1; k < 4; ++k) { a[0] = mfma(u[k], px[k][0], a[0]); a[1] = mfma(u[k], px[k][1], a[1]); }
    SB();
    if (__builtin_amdgcn_ballot_w64(max4(z[0]) == SENT) != 0) { v4u zz[1]; poll<1>(c, off, voff, zz); z[0] = zz[0]; }
    a[0] = mfma(z[0], z[1], a[0]); a[1] = mfma(z[0], z[2], a[1]);
}
DI void out_store(const Ctx& c, int t, int j, int m, f4 s) {
    const int l = lane_id();
    v2u o = {pk(s[0], s[1]), pk(s[2], s[3])};
    const __amdgpu_buffer_rsrc_t orr = __builtin_amdgcn_make_buffer_rsrc((void*)c.out, 0, 2048 * 2560 - 1280, 0x00020000);
    __builtin_amdgcn_raw_buffer_store_b64(o, orr, (l & 15) * 2560 + (l >> 4) * 8, (t * 32 + m * 16) * 2560 + j * 32, 0);
}
template <u32 LR, class F> DI void out_duty(Ctx& c, int t, int j, F mid) {
    f4 a[2];
    out_part(c, t, j, a, [] {});
    const u32 l16 = lane_id() * 16;
    mid();
    if (c.wave != 0) st128(LR + (c.wave - 1) * 1024 + l16, __builtin_bit_cast(v4u, a[0]));
    if (c.wave != 1) st128(LR + 7168 + (c.wave ? c.wave - 1 : 0) * 1024 + l16, __builtin_bit_cast(v4u, a[1]));
    bar();
    if (c.wave < 2) {
        f4 s = c.wave ? a[1] : a[0];
#pragma unroll
        for (int w = 0; w < 7; ++w) s += __builtin_bit_cast(f4, lds128(LR + c.wave * 7168 + w * 1024 + l16));
        out_store(c, t, j, c.wave, s);
    }
    bar();
}

template <bool WO> DI TB wbase(const Ctx& c, int l) {
    return mktb(WO ? 12288 + (l >> 4) * 1024 + (c.wave & 3) * 256 + rowp(l) : (c.wave >> 2) * 12288 + (l >> 4) * 3072 + (c.wave & 3) * 768 + rowp(l));
}
template <bool WO> DI void issue_chunk(const unsigned char* wb, u32 voff, int wave, int ch) {
    const u32 slot = ((ch + 2) % 6) * 24576;
#pragma unroll
    for (int u = 0; u < 3; ++u) {
        if (WO) dma(wb + (size_t)(3 * (ch & 1) + u) * 40960 + (ch >> 1) * 128, voff, slot + 12288 + (u * 4 + (wave & 3)) * 1024);
        else dma(wb + (size_t)ch * 393216 + u * 128, voff, slot + (wave >> 2) * 12288 + (wave & 3) * 3072 + u * 1024);
    }
}
template <bool WO> DI void load_weights(const Ctx& c, const u16* w, v8u (&W)[24]) {
    const int l = lane_id();
    const int kb = c.wave & 3, rr = ((l >> 1) & 7) ^ (4 * (kb & 1));
    const u32 voff = (8 * kb + rr) * (WO ? 1280 : 12288) + (l >> 4) * 32 + (l & 1) * 16;
    const unsigned char* wb = (const unsigned char*)w + (WO ? (size_t)c.h * 245760 : (size_t)c.h * 384);
    const TB wl = wbase<WO>(c, l);
    const size_t half = HALFSW * c.p * (WO ? (size_t)512 : (size_t)8 * 393216);
#pragma unroll
    for (int i = 0; i < 5; ++i) issue_chunk<WO>(wb + half, voff, c.wave, i);
#pragma unroll
    for (int i = 0; i < 16; ++i) {
        asm volatile("s_waitcnt vmcnt(12) lgkmcnt(0)\n\ts_barrier" ::: "memory");
        if (i < 15) issue_chunk<WO>(i + 5 < 8 ? wb + half : (i + 5 < 16 ? wb - half : wb), voff, c.wave, i + 5);
        const u32 slot = ((i + 2) % 6) * 24576;
#pragma unroll
        for (int u = 0; u < 3; ++u) {
            v4u f = trfrag(wl, slot + u * (WO ? 4096 : 256));
            const int ks = 3 * (i & 1) + u;
            const int pi = WO ? (i >> 1) * 3 + (ks >> 1) : u * 8 + (i >> 1), hf = WO ? (ks & 1) : (i & 1);
            W[pi] = hf ? cat8(lo4(W[pi]), f) : cat8(f, f);
        }
    }
    const u32 on = __builtin_amdgcn_readfirstlane(0u - (u32)c.p);
#pragma unroll
    for (int i = 0; i < 12 * HALFSW; ++i) {
        const int a = WO ? i : (i >> 2) * 8 + (i & 3), b = WO ? i + 12 : a + 4;
#pragma unroll
        for (int k = 0; k < 8; k += 2) {
            u32 x = W[a][k], y = W[b][k], z = W[a][k + 1], t = W[b][k + 1];
            asm volatile("s_mov_b32 exec_lo, %4\n\ts_mov_b32 exec_hi, %4\n\tv_swap_b32 %0, %1\n\tv_swap_b32 %2, %3\n\ts_mov_b64 exec, -1" : "+v"(x), "+v"(y), "+v"(z), "+v"(t) : "s"(on));
            W[a][k] = x; W[b][k] = y; W[a][k + 1] = z; W[b][k + 1] = t;
        }
    }
    barv();
}

template <u32 LM, class F> DI void crit_gemm1(Ctx& c, int s, const v8u (&W)[24], int l, u32 d01, u32 d2, u32 mi, F late) {
    TB wl = wbase<false>(c, l);
    v8u T[3];
#pragma unroll
    for (int u = 0; u < 3; ++u) T[u] = cat8(trfrag(wl, u * 256), trfrag(wl, 24576 + u * 256));
    wl.a = opq(wl.a + 49152); wl.b = opq(wl.b + 49152);
    const u32 idx = opq(0x4444u + (l & 8) * 0x1555u + ((l & 8) >> 2)), ab = opq(LM + l * 16);
    d01 = opq(d01); d2 = opq(d2);
    f4 acc[3] = {{0, 0, 0, 0}, {0, 0, 0, 0}, {0, 0, 0, 0}};
    asm volatile("" : "+v"(acc[0]), "+v"(acc[1]), "+v"(acc[2]));
    TS(c, 0);
    stage_mem<LM>(c, s, mi);
    TS(c, 1);
    v4u a[11];
    a[0] = lds128(ab + 8192);
    SB();
#pragma unroll
    for (int i = 1; i < 4; ++i) a[i] = lds128(ab + (i - 1) * 1024);
    SB();
#pragma unroll
    for (int i = 0; i < 10; ++i) {
#pragma unroll
        for (int u = 0; u < 3; ++u) acc[u] = smf(a[i], (i == 0 || i == 9) ? T[u] : W[u * 8 + i - 1], acc[u], idx);
        SB();
        if (i + 4 < 10) a[i + 4] = lds128(ab + (i + 4 == 9 ? 9 : i + 3) * 1024);
        if (i >= 3 && i < 6) T[i - 3] = cat8(trfrag(wl, (i - 3) * 256), trfrag(wl, 24576 + (i - 3) * 256));
        late(i);
        SB();
        if (LM == L_MEM && i == 0) TS2(c, 5);
    }
    if (LM == L_MEM) TS2(c, 6);
    float r[4];
#pragma unroll
    for (int i = 0; i < 4; ++i) r[i] = fold32(acc[0][i], acc[1][i]);
    st64(d01, v2u{pk(r[0], r[1]), pk(r[2], r[3])});
    st32(d2, pk(fold32(acc[2][0], acc[2][2]), fold32(acc[2][1], acc[2][3])));
    if (LM == L_MEM) TS2(c, 7);
}

DI constexpr int step_ks(int q) { return q < 8 ? ((q & 1) ? 16 + (q >> 1) : (q >> 1)) : q - 4; }
DI constexpr u32 step_slot(int q) { return (step_ks(q) >= 16 ? step_ks(q) - 16 : step_ks(q) + 4) * 2048; }
template <bool VM, class F> DI void bulk_gemm1(Ctx& c, const v8u (&W)[24], int s, F hook) {
    hook(-1);
    const int l = lane_id();
    const TB wl = wbase<false>(c, l);
    const u32 ab = opq((VM ? V_X : L_X) + xfrag(l));
    f4 acc[2][3];
#pragma unroll
    for (int m = 0; m < 2; ++m)
#pragma unroll
        for (int u = 0; u < 3; ++u) acc[m][u] = f4{0, 0, 0, 0};
    v4u a[22][2], b[5][3];
#pragma unroll
    for (int i = 0; i < 4; ++i) a[i >> 1][i & 1] = lds128(ab + step_slot(i >> 1) + (i & 1) * 1024);
#pragma unroll
    for (int u = 0; u < 3; ++u) b[0][u] = trfrag(wl, u * 256);
#pragma unroll
    for (int q = 0; q < 20; ++q) {
        const int ks = step_ks(q);
        if (VM && q == 10) {
            const int l2 = lane_id();
            const unsigned char* g = (const unsigned char*)c.x + (size_t)s * 40960 + 960;
            const u32 vo = (l2 & 15) * 1280 + (l2 >> 4) * 16;
            a[19][0] = *(const v4u*)(g + vo); a[19][1] = *(const v4u*)(g + 20480 + vo);
        }
        hook(q);
        SB();
#pragma unroll
        for (int m = 0; m < 2; ++m) {
#pragma unroll
            for (int u = 0; u < 3; ++u) {
                acc[m][u] = mfma(a[q][m], ks >= 16 ? b[ks - 16][u] : ((ks & 1) ? hi4(W[u * 8 + (ks >> 1)]) : lo4(W[u * 8 + (ks >> 1)])), acc[m][u]);
                if (m == 1 && ks >= 16 && ks < 19) { SB(); b[ks - 15][u] = trfrag(wl, (ks - 15) * 24576 + u * 256); SB(); }
            }
            SB();
            if (q < (VM ? 17 : 18)) a[q + 2][m] = lds128(ab + step_slot(q + 2) + m * 1024);
            SB();
        }
    }
    const int l3 = lane_id(), cc = l3 & 15;
    const u32 ia = L_IMG + (c.wave >> 2) * 12288 + ((c.wave & 3) * 48 + (cc ^ (4 * (cc >> 3)))) * 32 + (l3 >> 4) * 8;
    v2u v[2][3];
#pragma unroll
    for (int m = 0; m < 2; ++m)
#pragma unroll
        for (int u = 0; u < 3; ++u) v[m][u] = v2u{pk(acc[m][u][0], acc[m][u][1]), pk(acc[m][u][2], acc[m][u][3])};
    hook(20);
#pragma unroll
    for (int m = 0; m < 2; ++m)
#pragma unroll
        for (int u = 0; u < 3; ++u) st64(ia + m * 6144 + u * 512, v[m][u]);
}

DI bool out_vm(const Ctx& c) { return c.h >= 20 && c.h < 28; }
DI void vm_duty(Ctx& c, int s) {
    if (s + 6 < NSEG && c.wave >= 4) {
        load_x<2, 2>(c, s + 6, 16 + ((c.wave >> 1) & 1), c.wave & 1, V_XH);
        load_x<7, 2>(c, s + 6, (c.wave >> 1) & 1, c.wave & 1, V_XL);
        if (c.wave < 6) load_x<1, 1>(c, s + 6, 14, c.wave & 1, V_XL);
    }
    if (c.h >= 28 && s >= -2 && s + 8 < NSEG) prefetch_x(c, s + 8, V_DUMP);
    if (out_vm(c) && s >= 0) {
        flag_wait(c, s, true, 1);
        if (s + 2 >= NSEG) out_duty<V_MEM>(c, s, c.h + 12, [] {});
    }
}

template <bool MEM> DI void attn8(Ctx& c, int s) {
    const int l = lane_id(), g = l >> 4, cc = l & 15;
    const TB il = mktb(L_IMG + 12288 + g * 256 + rowp(l)), fl = mktb(L_F + g * 256 + (rowp(l) & ~16u));
    const u32 qo = L_IMG + 6144 - L_F + (c.wave & 1) * 16;
    const TB ql = MEM ? fl : TB{fl.a + qo, fl.b + qo};
    const u32 po = MEM ? mpb(c, s) : pxb(c, s) + (c.wave & 1) * 128;
    const u32 o1 = MEM ? po + MP_PX + io((c.h >> 1) * 1024) + (c.h & 1) * 128 : po + io(c.h * 2048 + 1024);
    const u32 o2 = MEM ? po + MP_PM + c.h * 128 : po + 131072 + io((c.h >> 2) * 2048 + 1024) + (c.h & 3) * 256;
    const u32 v1 = opq(g * 256 + (cc & 7) * 16 + (cc & 8)), v2 = opq(cc < 8 ? cc * 16 + (g & 1) * 8 + (g >> 1) * 4 : 0x80000000u);
    v4u k[6][2], q[6], m[6];
    constexpr int PF = 3;
#pragma unroll
    for (int ks = 0; ks < PF; ++ks) { k[ks][0] = trfrag(il, ks * 1024); k[ks][1] = trfrag(il, 6144 + ks * 1024); }
    asm volatile("" :: "s"(o1), "s"(o2));
    bar();
    TS(c, 2);
    q[0] = trfrag(ql, 0); q[1] = trfrag(ql, 1024); m[0] = trfrag(fl, 16);
    SB();
    f4 t[3] = {{0, 0, 0, 0}, {0, 0, 0, 0}, {0, 0, 0, 0}};
#pragma unroll
    for (int ks = 0; ks < 6; ++ks) {
        t[1] = mfma(k[ks][0], q[ks], t[1]);
        SB();
        if (ks < 5) m[ks + 1] = trfrag(fl, (ks + 1) * 1024 + 16);
        SB();
        t[2] = mfma(k[ks][1], q[ks], t[2]);
        SB();
        if (ks + PF < 6) { k[ks + PF][0] = trfrag(il, (ks + PF) * 1024); k[ks + PF][1] = trfrag(il, 6144 + (ks + PF) * 1024); }
        SB();
        t[0] = mfma(m[ks], q[ks], t[0]);
        SB();
        if (ks < 4) q[ks + 2] = trfrag(ql, (ks + 2) * 1024);
        SB();
    }
    const float sc = 0.07216878364870322f * 1.4426950408889634f;
    float v[5];
#pragma unroll
    for (int i = 0; i < 4; ++i) {
        v[i] = cc < 8 ? t[1][i] : t[2][i];
        if (!MEM) v[i] = (4 * g + i + 8 > (c.wave & 1) * 8 + cc && cc >= 8) ? __uint_as_float(opq(0xFF800000u)) : v[i];
    }
    v[4] = g < 2 ? (cc < 8 ? t[0][0] : t[0][1]) : (cc < 8 ? t[0][2] : t[0][3]);
    float mx = fmaxf(fmaxf(fmaxf(v[0], v[1]), fmaxf(v[2], v[3])), v[4]);
    mx = xmax(fmaxf(mx, ror8(mx))) * sc;
#pragma unroll
    for (int i = 0; i < 5; ++i) v[i] = __builtin_amdgcn_exp2f(__builtin_fmaf(v[i], sc, -mx));
    float sum = (v[0] + v[1]) + (v[2] + v[3]) + v[4];
    sum = xsum(sum + ror8(sum));
    const float inv = __builtin_amdgcn_rcpf(sum);
    gst64(c, o1, v1, v2u{pkc(v[0] * inv, v[1] * inv), pkc(v[2] * inv, v[3] * inv)});
    const float w = v[4] * inv;
    gst32(c, o2, v2, pkc(w, ror8(w)));
    __builtin_amdgcn_s_setprio(0);
}

DI void attn16(Ctx& c, int s) {
    const int l = lane_id(), g = l >> 4, cc = l & 15;
    const TB il = mktb(L_IMG + g * 256 + rowp(l));
    f4 t[2] = {{0, 0, 0, 0}, {0, 0, 0, 0}};
    v4u q[6], k[6], m[6];
#pragma unroll
    for (int ks = 0; ks < 4; ++ks) { q[ks] = trfrag(il, ks * 1024); k[ks] = trfrag(il, 12288 + ks * 1024); }
    bar();
    TS(c, 2);
    m[0] = trfrag(il, L_F - L_IMG); m[1] = trfrag(il, L_F - L_IMG + 1024);
    SB();
#pragma unroll
    for (int ks = 0; ks < 6; ++ks) {
        t[1] = mfma(k[ks], q[ks], t[1]); t[0] = mfma(m[ks], q[ks], t[0]);
        SB();
        if (ks < 4) m[ks + 2] = trfrag(il, L_F - L_IMG + (ks + 2) * 1024);
        if (ks < 2) { q[ks + 4] = trfrag(il, (ks + 4) * 1024); k[ks + 4] = trfrag(il, 12288 + (ks + 4) * 1024); }
        SB();
    }
    const float sc = 0.07216878364870322f * 1.4426950408889634f, ninf = __uint_as_float(opq(0xFF800000u));
    float mx = ninf;
#pragma unroll
    for (int k = 0; k < 2; ++k)
#pragma unroll
        for (int i = 0; i < 4; ++i) {
            const bool vis = k == 0 ? g >= 2 : (4 * g + i <= cc);
            t[k][i] = vis ? t[k][i] : ninf; mx = fmaxf(mx, t[k][i]);
        }
    mx = xmax(mx) * sc;
    float sum = 0;
#pragma unroll
    for (int k = 0; k < 2; ++k)
#pragma unroll
        for (int i = 0; i < 4; ++i) { t[k][i] = __builtin_amdgcn_exp2f(__builtin_fmaf(t[k][i], sc, -mx)); sum += t[k][i]; }
    sum = xsum(sum);
    const float inv = __builtin_amdgcn_rcpf(sum);
    const v4u px = {pk(t[1][0] * inv, t[1][1] * inv), pk(t[1][2] * inv, t[1][3] * inv), 0u, 0u};
    const v2u pm = {pk(t[0][0] * inv, t[0][1] * inv), pk(t[0][2] * inv, t[0][3] * inv)};
    const u32 po = pxb(c, s);
    gst(c, po + io(c.h * 2048), l * 16, px);
    if (g >= 2) gst64(c, po + 131072 + io((c.h >> 2) * 2048) + (c.h & 3) * 256, cc * 16 + (g & 1) * 8, pm);
    __builtin_amdgcn_s_setprio(0);
}
DI void load_x_main(const Ctx& c, int s) {
    const int pw = (c.wave >> 1) ^ 2;
    load_x<1, 1>(c, s, 16 + pw, c.wave & 1, L_XH);
    load_x<2, 1>(c, s, 2 * pw, c.wave & 1, L_XL);
}

DI void qk_main(Ctx& c, const u16* wq, const u16* wk) {
    v8u W[24];
    load_x<2, 4>(c, c.p, 8 + (c.wave >> 1), c.wave & 1, L_XL);
    load_weights<false>(c, c.wave < 4 ? wq : wk, W);
    if (c.par == 0 && c.p == 1 && c.h < NPAGE) { if (c.wave == 0) probe_page(c, c.h); bar(); }
    load_x_main(c, c.p);
    barv();
    for (int s = c.p - 2; s < NSEG; s += 2) {
        asm volatile("" : "+s"(c.wave), "+s"(c.h), "+s"(c.par));
        if (s >= 0) {
            const int l = lane_id(), g = l >> 4, cc = l & 15;
            const u32 fa = L_F + ((c.wave & 3) * 48 + (cc ^ (4 * (cc >> 3)))) * 32 + (c.wave >> 2) * 16 + (g & 1) * 8;
            if (c.wave == 0) __builtin_amdgcn_s_setprio(3);
            else if (c.wave < 4) __builtin_amdgcn_s_setprio(1);
            crit_gemm1<L_MEM>(c, s, W, l, fa + (g >> 1) * 512, fa + 1024 + (g >> 1) * 4, mem_in(c, s), [](int) {});
            if (c.wave == 0) attn8<true>(c, s);
            else if (c.wave == 1) attn16(c, s);
            else if (c.wave < 4) attn8<false>(c, s);
            else {
                bar();
                TS(c, 2);
                if (c.wave >= 6 && s + 2 < NSEG) load_x<5, 1>(c, s + 2, 8, c.wave & 1, L_XL);
                if (c.wave == 4) __builtin_amdgcn_s_sleep(QSLEEP);
            }
            TS(c, 3);
            if (s < 2) {
                const v4u sv = sentv();
                for (int i = c.wave * 64 + lane_id(); i < 30 * 40; i += 512) {
                    const int st = c.p + 4 + 2 * (i / 40), e = i % 40;
                    const u32 o = e < 32 ? MP_PX + io((c.h >> 1) * 1024) + ((e >> 3) * 16 + (c.h & 1) * 8 + (e & 7)) * 16 : MP_PM + c.h * 128 + (e & 7) * 16;
                    gst(c, 0, mpb(c, st) + o, sv);
                }
            }
        }
        auto flag_p = [&] { gst32(c, fb(c, s < 0 ? 0 : s) + 512 + c.h * 4, c.wave == 7 && s >= 0 ? 0u : 0x80000000u, opq(1u)); };
        if (s + 2 < NSEG) {
            f4 a[2];
            if (c.wave < 4) {
                bar();
                TS1(c, 5);
                if (c.wave < 2 && s >= 0) load_x<3, 1>(c, s + 2, 13, c.wave & 1, L_XL);
                if (s >= 2) {
                    out_part(c, s - 2, c.h, a, [] { asm volatile("s_waitcnt vmcnt(12)\n\ts_barrier" ::: "memory"); });
                    const u32 pa = L_IMG + c.wave * 2048 + lane_id() * 16;
                    st128(pa, __builtin_bit_cast(v4u, a[0])); st128(pa + 1024, __builtin_bit_cast(v4u, a[1]));
                } else barv();
                TS1(c, 6);
            }
            bulk_gemm1<false>(c, W, s + 2, [&](int q) {
                if (q == QM1) bar_half<true, true>(c.wave);
                if (q == QM0) bar_half<false, true>(c.wave);
                if (q == 15) bar_half<false, false>(c.wave);
                if (q == 20) { TS1(c, 7); bar_half<true, false>(c.wave); }
            });
            if (c.wave >= 4) {
                TS1(c, 5);
                if (s >= 2) {
                    out_part(c, s - 2, c.h, a, [&] { asm volatile("s_barrier" ::: "memory"); flag_p(); });
                    const u32 l16 = lane_id() * 16, pa = L_IMG + (c.wave - 4) * 2048 + l16;
                    a[0] += __builtin_bit_cast(f4, lds128(pa)); a[1] += __builtin_bit_cast(f4, lds128(pa + 1024));
                    const u32 pb = L_X + (c.wave >= 6 ? c.wave - 2 : 0) * 2048 + l16;
                    if (c.wave != 4) st128(pb, __builtin_bit_cast(v4u, a[0]));
                    if (c.wave != 5) st128(pb + 1024, __builtin_bit_cast(v4u, a[1]));
                    TS1(c, 6);
                    bar();
                    if (c.wave < 6) {
                        const u32 pc = L_X + (c.wave & 1) * 1024 + l16;
                        f4 r = (c.wave & 1) ? a[1] : a[0];
                        r += __builtin_bit_cast(f4, lds128(pc));
                        r += __builtin_bit_cast(f4, lds128(pc + 8192)) + __builtin_bit_cast(f4, lds128(pc + 10240));
                        out_store(c, s - 2, c.h, c.wave & 1, r);
                    }
                } else { bar(); flag_p(); bar(); }
            }
            TS(c, 4);
            if (s + 4 < NSEG) load_x_main(c, s + 4);
            if (s < 0) get_mask(c);
            else flag_wait(c, s, false, 1);
        } else {
            barv(); flag_p();
            for (int t = s - 2; t <= s; t += 2) { flag_wait(c, t, false, 1); out_duty<L_MEM>(c, t, c.h, [] {}); }
        }
        TSF(c, s);
    }
}

DI u32 wo_off(int i, int ks) { return ((i - 8) * 2 + ks / 3) * 24576 + (ks % 3) * 4096; }
DI u32 z_lane(int l) { const int g = l >> 4; return ((g >> 1) + 2 * (g & 1)) * 65536 + (l & 15) * 16; }
DI void swap32(f4& a, f4& b, int i) {
    auto s = __builtin_amdgcn_permlane32_swap(__float_as_uint(a[i]), __float_as_uint(b[i]), false, false);
    a[i] = __uint_as_float(s[0]); b[i] = __uint_as_float(s[1]);
}
DI v4u z_pack(const u32 (&x)[2], const u32 (&y)[2]) {
    auto s0 = __builtin_amdgcn_permlane16_swap(x[0], y[0], false, false);
    auto s1 = __builtin_amdgcn_permlane16_swap(x[1], y[1], false, false);
    return v4u{s0[0], s1[0], s0[1], s1[1]};
}
template <class F> DI void z_tail(Ctx& c, u32 zo, TB wl, TB wh, TB il, F mid) {
    v4u a[3]; v8u T[2][3];
    asm volatile("" ::: "memory");
    SB();
    T[0][0] = cat8(trfrag(wl, wo_off(8, 0)), trfrag(wl, wo_off(8, 1))); T[1][0] = cat8(trfrag(wh, wo_off(8, 0)), trfrag(wh, wo_off(8, 1)));
    asm volatile("s_waitcnt lgkmcnt(8)\n\ts_barrier" ::: "memory");
    TS(c, 2);
#pragma unroll
    for (int f = 0; f < 3; ++f) a[f] = trfrag(il, f * 1024);
    SB();
    const int l = lane_id(), g = l >> 4;
    const u32 idx = 0x4444u + (l & 8) * 0x1555u + ((l & 8) >> 2);
    f4 acc[2] = {{0, 0, 0, 0}, {0, 0, 0, 0}};
    mid();
    SB();
#pragma unroll
    for (int f = 0; f < 3; ++f)
#pragma unroll
        for (int j = 0; j < 2; ++j) {
            acc[j] = smf(a[f], T[j][f], acc[j], idx);
            SB();
            if (f < 2) T[j][f + 1] = cat8(trfrag(j ? wh : wl, wo_off(8, 2 * f + 2)), trfrag(j ? wh : wl, wo_off(8, 2 * f + 3)));
            SB();
        }
    const u32 x[2] = {pkc(fold32(acc[0][0], acc[1][0]), fold32(acc[0][1], acc[1][1])), pkc(fold32(acc[0][2], acc[1][2]), fold32(acc[0][3], acc[1][3]))};
    gst(c, zo, (g & 1) ? 0x80000000u : z_lane(l), z_pack(x, x));
    __builtin_amdgcn_s_setprio(0);
}

DI void v_main(Ctx& c, const u16* wv) {
    v8u W[24];
    load_weights<false>(c, wv, W);
    NodeA na = {};
    for (int s = c.p - 4; s < NSEG; s += 2) {
        asm volatile("" : "+s"(c.wave), "+s"(c.h), "+s"(c.par));
        if (s >= 0) {
            const int l = lane_id(), g = l >> 4, cc = l & 15;
            const int rho = ((cc >> 2) << 1) | (cc & 1);
            const u32 a = V_VS + ((cc >> 1) & 1) * 16 + (g & 1) * 8;
            const int t0 = c.wave * 3 + (g >> 1), t2 = c.wave * 3 + 2;
            TB wl, wh, il;
            const u32 pa = opq((12288 + g * 1024 + c.wave * 256 + rowp(l)) | (g * 256 + rowp(l)) << 16);
            __builtin_amdgcn_s_setprio(VPRIO);
            crit_gemm1<V_MEM>(c, s, W, l, a + (t0 * 8 + (rho ^ (4 * (t0 & 1)))) * 32, a + (t2 * 8 + (rho ^ (4 * (t2 & 1)))) * 32 + (g >> 1) * 4,
                na.mi, [&](int i) {
                    if (i == 7) { wl = mktb(pa & 0xFFFFu); il.a = opq(V_VS + (pa >> 16)); }
                    if (i == 8) { il.b = opq(il.a ^ 128u); wh = TB{opq(wl.a + 49152), opq(wl.b + 49152)}; }
                });
            v4u u[2][4];
            z_tail(c, na.zo + 2 * 262144, wl, wh, il, [&] { node_u<0, VPRE>(c, na, u); });
            TS(c, 6);
            if (out_vm(c) && s >= 2) {
                f4 a[2];
                __builtin_amdgcn_s_sleep(OVSLEEP);
                out_part(c, s - 2, c.h + 12, a, [] {});
                const u32 pa = V_MEM + c.wave * 2048 + lane_id() * 16;
                st128(pa, __builtin_bit_cast(v4u, a[0])); st128(pa + 1024, __builtin_bit_cast(v4u, a[1]));
                TS(c, 5);
            } else if (c.h >= 20) __builtin_amdgcn_s_sleep(VSLEEP);
            else if (s < NSEG - 1) { node_u<VPRE, 8>(c, na, u); node_duty<NSLEEP + NVX>(c, na, u); }
            if (c.h < 20 && s < 2) {
                const v4u sv = sentv();
                for (int i = c.wave * 64 + lane_id(); i < 30 * 32; i += 256) {
                    const int st = 5 + c.p + 2 * (i >> 5);
                    if (st < NSEG) gst(c, 0, mpb(c, st) + io(c.h * 512) + (i & 31) * 16, sv);
                }
            }
        } else {
            barv();
            if (s >= -2) get_mask(c);
        }
        if (s + 4 < NSEG) bulk_gemm1<true>(c, W, s + 4, [&](int q) {
            if (q == 9) na = node_addr(c, (s + 2) & 63);
            if (q == 20) { TS(c, 3); bar(); }
        });
        else { na = node_addr(c, (s + 2) & 63); bar(); }
        if (out_vm(c) && s >= 2) bar();
        TS(c, 4);
        vm_duty(c, s);
        TSF(c, s);
    }
}

DI void bulk_gemm2(const Ctx& c, const v8u (&W)[24], int s) {
    const int l = lane_id();
    const TB wl = wbase<true>(c, l), il = mktb(L_IMG + (l >> 4) * 256 + rowp(l));
    const u32 uo = ub(c, s, c.wave & 3) + io(c.h * 1024);
    v4u a[20][2], b[20];
#pragma unroll
    for (int i = 0; i < 4; ++i) a[i >> 1][i & 1] = trfrag(il, (i & 1) * 6144 + (i >> 1) * 1024);
#pragma unroll
    for (int ig = 0; ig < 3; ++ig) {
        const int nt = ig == 0 ? 4 : 3;
        f4 acc[2][4];
#pragma unroll
        for (int j = 0; j < 4; ++j) { acc[0][j] = f4{0, 0, 0, 0}; acc[1][j] = f4{0, 0, 0, 0}; }
#pragma unroll
        for (int ks = 0; ks < 6; ++ks) {
            const int st = ig * 6 + ks;
#pragma unroll
            for (int m = 0; m < 2; ++m) {
                SB();
#pragma unroll
                for (int j = 0; j < nt; ++j) {
                    const int i = ig == 0 ? j : (j < 2 ? 2 + 2 * ig + j : 7 + ig);
                    acc[m][j] = mfma(a[st][m], i < 8 ? ((ks & 1) ? hi4(W[i * 3 + (ks >> 1)]) : lo4(W[i * 3 + (ks >> 1)])) : b[st], acc[m][j]);
                    if (m == 0 && j == 0 && st >= 5 && st < 17) { SB(); b[st + 1] = trfrag(wl, wo_off(7 + (st + 1) / 6, (st + 1) % 6)); SB(); }
                }
                SB();
                if (st < 16) a[st + 2][m] = trfrag(il, m * 6144 + ((st + 2) % 6) * 1024);
            }
        }
        SB();
#pragma unroll
        for (int j = 0; j < nt; ++j) {
            const int i = ig == 0 ? j : (j < 2 ? 2 + 2 * ig + j : 7 + ig);
            const v4u v = {pk(acc[0][j][0], acc[0][j][1]), pk(acc[0][j][2], acc[0][j][3]), pk(acc[1][j][0], acc[1][j][1]), pk(acc[1][j][2], acc[1][j][3])};
            gst(c, uo + i * 65536, l * 16, v);
        }
        SB();
    }
}

DI void o_main(Ctx& c, const u16* wo) {
    v8u W[24];
    load_weights<true>(c, wo, W);
    load_x<2, 2>(c, c.p, 16 + ((c.wave >> 1) & 1), c.wave & 1, V_XH);
    load_x<4, 2>(c, c.p, (c.wave >> 1) & 1, c.wave & 1, V_XL);
    for (int s = c.p - 4; s < NSEG; s += 2) {
        asm volatile("" : "+s"(c.wave), "+s"(c.h), "+s"(c.par));
        if (s >= 0) {
            const int l = lane_id(), g = l >> 4;
            const TB il = mktb(V_VS + g * 256 + rowp(l));
            const u32 idx = opq(0x4444u + (l & 8) * 0x1555u + ((l & 8) >> 2)), za = opq(z_lane(l));
            const NodeA na = node_addr(c, s);
            u32 zo = na.zo, zo1 = zo + 262144;
            asm volatile("" : "+s"(zo1));
            stage_mem<V_MEM>(c, s, na.mi);
            v4u u[2][4];
            if (c.h < 20) flag_wait(c, s, true, 1);
            node_u<0, UPRE>(c, na, u);
            f4 acc[8];
#pragma unroll
            for (int i = 0; i < 4; ++i) { acc[i] = f4{0, 0, 0, 0}; asm volatile("" : "+v"(acc[i])); }
            __builtin_amdgcn_s_setprio(OPRIO);
            TS2(c, 6);
            bar();
            TS(c, 2);
            v4u a[3];
#pragma unroll
            for (int f = 0; f < 3; ++f) a[f] = trfrag(il, f * 1024);
            SB();
            u32 xp[4][2];
#pragma unroll
            for (int pr = 0; pr < 5; ++pr) {
                float x[4];
#pragma unroll
                for (int n = 0; n < 6; ++n) {
                    if (pr < 4) acc[2 * pr + (n & 1)] = smf(a[n >> 1], W[(2 * pr + (n & 1)) * 3 + (n >> 1)], acc[2 * pr + (n & 1)], idx);
                    SB();
                    if (pr > 0) {
                        f4& p = acc[2 * pr - 2]; f4& q = acc[2 * pr - 1];
                        if (n == 1) { swap32(p, q, 0); swap32(p, q, 1); }
                        if (n == 2) { swap32(p, q, 2); swap32(p, q, 3); x[0] = opqf(p[0] + q[0]); }
                        if (n == 3) { x[1] = opqf(p[1] + q[1]); x[2] = opqf(p[2] + q[2]); x[3] = opqf(p[3] + q[3]); }
                        if (n == 4) { xp[pr - 1][0] = pkc(x[0], x[1]); xp[pr - 1][1] = pkc(x[2], x[3]); }
                        if (n == 5 && !(pr & 1)) gst(c, pr == 2 ? zo : zo1, za, z_pack(xp[pr - 2], xp[pr - 1]));
                        if (n >= 4 && pr < 3) { f4& z = acc[2 * pr + n - 2]; z = f4{0, 0, 0, 0}; asm volatile("" : "+v"(z)); }
                    }
                    SB();
                }
            }
            __builtin_amdgcn_s_setprio(0);
            TS(c, 3);
            if (c.h < 20 && s < NSEG - 1) { node_u<UPRE, 8>(c, na, u); node_duty<NSLEEP>(c, na, u); }
        } else {
            barv();
            if (s >= -2) get_mask(c);
        }
        const bool bulk = s >= -2 && s + 2 < NSEG;
        if (bulk) {
            __builtin_amdgcn_s_setprio(UPRIO);
            bulk_gemm2(c, W, s + 2);
            __builtin_amdgcn_s_setprio(0);
            const int l = lane_id(), g = l >> 4;
            const v4u sv = sentv();
            const u32 za = z_lane(l);
#pragma unroll
            for (int k = 0; k < 3; ++k)
                if (k < 2 || !(g & 1)) gst(c, zb(c, s + 2) + k * 262144 + (c.wave & 3) * 16384 + c.h * 256, za, sv);
        }
        auto flag_u = [&] { if (bulk) { asm volatile("s_waitcnt vmcnt(0)" ::: "memory"); gst32(c, fb(c, s + 2) + (c.wave & 3) * 128 + c.h * 4, 0, opq(1u)); } };
        if (out_vm(c) && s >= 2) {
            f4 a[2];
            TS(c, 5);
            out_part(c, s - 2, c.h + 12, a, [] {});
            flag_u();
            TS(c, 0);
            bar();
            TS(c, 1);
            const u32 l16 = lane_id() * 16, pa = V_MEM + (c.wave - 4) * 2048 + l16;
            a[0] += __builtin_bit_cast(f4, lds128(pa)); a[1] += __builtin_bit_cast(f4, lds128(pa + 1024));
            const u32 pb = V_X + 4096 + (c.wave >= 6 ? c.wave - 5 : 0) * 8192 + l16;
            if (c.wave != 4) st128(pb, __builtin_bit_cast(v4u, a[0]));
            if (c.wave != 5) st128(pb + 1024, __builtin_bit_cast(v4u, a[1]));
            bar();
            if (c.wave < 6) {
                const u32 pc = V_X + 4096 + (c.wave & 1) * 1024 + l16;
                f4 r = (c.wave & 1) ? a[1] : a[0];
                r += __builtin_bit_cast(f4, lds128(pc));
                r += __builtin_bit_cast(f4, lds128(pc + 8192)) + __builtin_bit_cast(f4, lds128(pc + 16384));
                out_store(c, s - 2, c.h + 12, c.wave & 1, r);
            }
        } else {
            TS(c, 0);
            bar();
            TS(c, 1);
            flag_u();
        }
        TS(c, 4);
        vm_duty(c, s);
        TSF(c, s);
    }
}

__global__ void __launch_bounds__(512, 2) memtr_kernel(Args a) {
    Ctx c;
    const int wg = blockIdx.x;
    c.wave = __builtin_amdgcn_readfirstlane(threadIdx.x >> 6);
    const int br = (wg & 7) >> 2;
    const int idx = (wg >> 3) * 4 + (wg & 3);
    c.h = idx & 31; c.p = idx >> 6;
    c.r = __builtin_amdgcn_make_buffer_rsrc(a.ws, 0, WS_SPAN, 0x00020000);
    c.par = br ? ~0u : 0u;
    c.spin = 0;
    c.x = br ? a.x[1] : a.x[0]; c.mem0 = br ? a.mem0[1] : a.mem0[0]; c.out = a.out + br * 640;
    if (((idx >> 5) & 1) == 0) qk_main(c, br ? a.w[1][0] : a.w[0][0], br ? a.w[1][1] : a.w[0][1]);
    else {
        if (c.h >= 28) { prefetch_x(c, c.p + 2, V_XL + 7 * 2048); prefetch_x(c, c.p + 4, V_XL + 7 * 2048); }
        if (c.wave < 7) { load_x<1, 1>(c, c.p, 8 + c.wave, 0, V_XL); load_x<1, 1>(c, c.p, 8 + c.wave, 1, V_XL); }
        if (c.wave < 4) v_main(c, br ? a.w[1][2] : a.w[0][2]);
        else o_main(c, br ? a.w[1][3] : a.w[0][3]);
    }
}

extern "C" void kernel_launch(void* const* d_in, const int* in_sizes, int n_in, void* d_out, int out_size, void* d_ws, size_t ws_size, hipStream_t stream) {
    Args a;
    for (int b = 0; b < 2; ++b) {
        a.x[b] = (const u16*)d_in[7 * b]; a.mem0[b] = (const u16*)d_in[7 * b + 1];
        for (int k = 0; k < 4; ++k) a.w[b][k] = (const u16*)d_in[7 * b + 2 + k];
    }
    a.out = (u16*)d_out; a.ws = (unsigned char*)d_ws;
    (void)hipMemsetAsync((unsigned char*)d_ws + INIT_OFF, 0xFF, INIT_BYTES, stream);
    memtr_kernel<<<256, 512, 0, stream>>>(a);
}
```

```cpp
#include <hip/hip_runtime.h>
#include <stdint.h>

typedef unsigned u32;
typedef unsigned short u16;
typedef u32 v2u __attribute__((ext_vector_type(2)));
typedef u32 v4u __attribute__((ext_vector_type(4)));
typedef u32 v8u __attribute__((ext_vector_type(8)));
typedef float f4 __attribute__((ext_vector_type(4)));
typedef float f2 __attribute__((ext_vector_type(2)));
typedef short s4 __attribute__((ext_vector_type(4)));
typedef __bf16 b2 __attribute__((ext_vector_type(2)));
typedef __bf16 b8 __attribute__((ext_vector_type(8)));
typedef __bf16 b16 __attribute__((ext_vector_type(16)));
#define AS3 __attribute__((address_space(3)))
#define DI __device__ __forceinline__

constexpr int NSEG = 64;
constexpr u32 SENT = 0xFFFFFFFFu;
constexpr int NPAGE = 17;
constexpr u32 WS_SPAN = NPAGE * 2097152;
constexpr u32 INIT_OFF = 2097152 - 147456, INIT_BYTES = 147456 + 5 * 65536;
constexpr u32 MP_PM = 20480, MP_PX = 32768;
constexpr u32 L_IMG = 98304, L_X = 122880, L_MEM = L_X + 24576, L_F = L_X + 34816;
constexpr u32 V_MEM = L_IMG + 12288, V_VS = V_MEM + 10240, V_DUMP = V_VS + 3072, V_X = V_DUMP + 1024;
#define QSLEEP 5
#define UPRIO 1
#define QM0 7
#define QM1 1
#define NSLEEP 5
#define OSLEEP 0
#define OVSLEEP 0
#define UPRE 6
#define VPRE 4
#define VPRIO 3
#define OPRIO 3
#define PSLEEP 3
#ifndef NVX
#define NVX 3
#endif
#define VSLEEP 0
constexpr u32 SPIN_MAX = 1u << 20;

struct Args { const u16* x[2]; const u16* mem0[2]; const u16* w[2][4]; u16* out; unsigned char* ws; };

__shared__ __attribute__((aligned(1024))) unsigned char lds[163840];

DI v4u lds128(u32 a) { return *(AS3 v4u*)((AS3 unsigned char*)lds + a); }
DI f2 lds64f(u32 a) { return *(AS3 f2*)((AS3 unsigned char*)lds + a); }
DI void st128(u32 a, v4u v) { *(AS3 v4u*)((AS3 unsigned char*)lds + a) = v; }
DI void st64(u32 a, v2u v) { *(AS3 v2u*)((AS3 unsigned char*)lds + a) = v; }
DI void st32(u32 a, u32 v) { *(AS3 u32*)((AS3 unsigned char*)lds + a) = v; }
DI v2u ldtr(u32 a) { return __builtin_bit_cast(v2u, __builtin_amdgcn_ds_read_tr16_b64_v4i16((AS3 s4*)((AS3 unsigned char*)lds + a))); }
struct TB { u32 a, b; };
DI u32 opq(u32 v);
DI TB mktb(u32 a) { TB t = {opq(a), opq(a ^ 128u)}; return t; }
DI v4u trfrag(TB t, u32 off) { v2u lo = ldtr(t.a + off), hi = ldtr(t.b + off); v4u r = {lo.x, lo.y, hi.x, hi.y}; return r; }
DI int lane_id() { int l; asm volatile("v_mbcnt_lo_u32_b32 %0, -1, 0\n\tv_mbcnt_hi_u32_b32 %0, -1, %0" : "=v"(l)); return l; }
DI u32 opq(u32 v) { asm volatile("" : "+v"(v)); return v; }
DI float opqf(float v) { asm volatile("" : "+v"(v)); return v; }
DI v4u sentv() { const u32 v = opq(SENT); v4u r = {v, v, v, v}; return r; }
DI u32 rowp(int l) { return ((((l & 15) >> 2) ^ ((l >> 2) & 4)) * 32 + (l & 3) * 8); }
DI v8u cat8(v4u a, v4u b) { return __builtin_shufflevector(a, b, 0, 1, 2, 3, 4, 5, 6, 7); }
DI v4u lo4(v8u a) { return __builtin_shufflevector(a, a, 0, 1, 2, 3); }
DI v4u hi4(v8u a) { return __builtin_shufflevector(a, a, 4, 5, 6, 7); }
DI f4 mfma(v4u a, v4u b, f4 c) { return __builtin_amdgcn_mfma_f32_16x16x32_bf16(__builtin_bit_cast(b8, a), __builtin_bit_cast(b8, b), c, 0, 0, 0); }
DI f4 smf(v4u a, v8u b, f4 c, u32 idx) { return __builtin_amdgcn_smfmac_f32_16x16x64_bf16(__builtin_bit_cast(b8, a), __builtin_bit_cast(b16, b), c, (int)idx, 0, 0); }
DI u32 pk(float a, float b) { b2 r = {(__bf16)a, (__bf16)b}; return __builtin_bit_cast(u32, r); }
DI u32 pkc(float a, float b) { u32 r = pk(a, b); return r < 0xFFFFFFFEu ? r : 0xFFFFFFFEu; }
DI void bar() { asm volatile("s_waitcnt lgkmcnt(0)\n\ts_barrier" ::: "memory"); }
DI void barv() { asm volatile("s_waitcnt vmcnt(0) lgkmcnt(0)\n\ts_barrier" ::: "memory"); }
template <bool LOW, bool VMEM> DI void bar_half(int wave) {
    if (VMEM) asm volatile("s_cmp_lt_u32 %0, 4\n\ts_cbranch_scc%1 LBH%=\n\ts_waitcnt vmcnt(0) lgkmcnt(0)\n\ts_barrier\nLBH%=:" :: "s"(wave), "n"(LOW ? 0 : 1) : "memory", "scc");
    else asm volatile("s_cmp_lt_u32 %0, 4\n\ts_cbranch_scc%1 LBH%=\n\ts_waitcnt lgkmcnt(0)\n\ts_barrier\nLBH%=:" :: "s"(wave), "n"(LOW ? 0 : 1) : "memory", "scc");
}
#define WHINT " nt"
#define XHINT ""
#define PROUNDS 3
#define QB 7
#define MPF 3
#define MPRE 1
#define BSLEEP 0
#define PFV 1
#define PF0 1
#define HALFSW 0
DI void dma(const void* sbase, u32 voff, u32 ldst) {
    asm volatile("s_mov_b32 m0, %2\n\ts_nop 0\n\tglobal_load_lds_dwordx4 %0, %1" WHINT :: "v"(voff), "s"(sbase), "s"(ldst) : "memory");
}
template <int GO, int LO> DI void dmai(const void* sbase, u32 voff, u32 ldst) {
    asm volatile("s_add_u32 m0, %2, %3\n\ts_nop 0\n\tglobal_load_lds_dwordx4 %0, %1 offset:%4" XHINT :: "v"(voff), "s"(sbase), "s"(ldst), "n"(LO - GO), "n"(GO) : "memory", "scc");
}
DI float fold32(float a, float b) { auto s = __builtin_amdgcn_permlane32_swap(__float_as_uint(a), __float_as_uint(b), false, false); return __uint_as_float(s[0]) + __uint_as_float(s[1]); }
DI float fmx(float a, float b) { return __builtin_elementwise_maximum(a, b); }
DI float xmax(float x) {
    auto s = __builtin_amdgcn_permlane16_swap(__float_as_uint(x), __float_as_uint(x), false, false); x = fmx(__uint_as_float(s[0]), __uint_as_float(s[1]));
    auto t = __builtin_amdgcn_permlane32_swap(__float_as_uint(x), __float_as_uint(x), false, false); return fmx(__uint_as_float(t[0]), __uint_as_float(t[1]));
}
DI float xsum(float x) {
    auto s = __builtin_amdgcn_permlane16_swap(__float_as_uint(x), __float_as_uint(x), false, false); x = __uint_as_float(s[0]) + __uint_as_float(s[1]);
    auto t = __builtin_amdgcn_permlane32_swap(__float_as_uint(x), __float_as_uint(x), false, false); return __uint_as_float(t[0]) + __uint_as_float(t[1]);
}
DI float ror8(float x) { return __uint_as_float(__builtin_amdgcn_update_dpp(0, __float_as_uint(x), 0x128, 0xF, 0xF, true)); }
DI u32 umax(u32 a, u32 b) { return a > b ? a : b; }
DI u32 max4(v4u v) { return umax(umax(v.x, v.y), umax(v.z, v.w)); }

struct Ctx {
    __amdgpu_buffer_rsrc_t r; u32 par; int h, p, wave; u32 spin;
    const u16* x; const u16* mem0; u16* out;
};
DI constexpr u32 io(u32 o) { return ((o >> 13) << 14) | (o & 8191u); }
DI u32 pgb(const Ctx& c, u32 page) { return (page << 21) | (((c.par >> page) & 1u) << 13); }
DI u32 mpb(const Ctx& c, int s) { return pgb(c, 1 + ((u32)s >> 5)) + ((u32)s & 31u) * 65536u; }
DI u32 zb(const Ctx& c, int s) { const u32 b = (u32)s & 7u, q = (b * 11u) >> 5; return pgb(c, 3 + q) + (b - 3u * q) * 655360u; }
DI u32 ub(const Ctx& c, int s, u32 w) { const u32 b = ((u32)s & 7u) * 4u + w, q = (b * 43u) >> 7; return pgb(c, 6 + q) + (b - 3u * q) * 655360u; }
DI u32 pxb(const Ctx& c, int s) { return pgb(c, 0) + ((u32)s % 6u) * 163840u; }
DI u32 fb(const Ctx& c, int s) { return pgb(c, 0) + (INIT_OFF + 16384) + io((u32)s * 1024u); }
DI v4u gld(const Ctx& c, u32 off, u32 voff) { return __builtin_amdgcn_raw_buffer_load_b128(c.r, voff, off, 16); }
DI void gst(const Ctx& c, u32 off, u32 voff, v4u v) { __builtin_amdgcn_raw_buffer_store_b128(v, c.r, voff, off, 16); }
DI void gst64(const Ctx& c, u32 off, u32 voff, v2u v) { __builtin_amdgcn_raw_buffer_store_b64(v, c.r, voff, off, 16); }
DI void gst32(const Ctx& c, u32 off, u32 voff, u32 v) { __builtin_amdgcn_raw_buffer_store_b32(v, c.r, voff, off, 16); }

#define TS(c, k)
#define TSF(c, s)
#define TS1(c, k) TS(c, k)
#define TS2(c, k)
#define SB() __builtin_amdgcn_sched_barrier(0)

template <int N> DI void poll(Ctx& c, const u32 (&off)[N], const u32 (&voff)[N], v4u (&v)[N]) {
    for (;;) {
        asm volatile("" ::: "memory");
        u32 m = 0;
#pragma unroll
        for (int k = 0; k < N; ++k) v[k] = gld(c, off[k], voff[k]);
#pragma unroll
        for (int k = 0; k < N; ++k) m = umax(m, max4(v[k]));
        if (__builtin_amdgcn_ballot_w64(m == SENT) == 0) return;
        if (++c.spin > SPIN_MAX) return;
    }
}

DI void probe_prep(const Ctx& c, int pg) {
    const int l = lane_id();
    __builtin_amdgcn_raw_buffer_store_b32(opq(SENT), c.r, (l & 1) * 8192 + ((l >> 1) & 3) * 256, pg * 2097152 + 1048576, 16);
}
DI void probe_page(const Ctx& c, int pg) {
    u32 best[2] = {~0u, ~0u};
    const u32 o = pg * 2097152 + 1048576;
    for (int r = 0; r < PROUNDS; ++r) {
#pragma unroll
        for (int k = 0; k < 2; ++k) {
            const u32 t0 = (u32)__builtin_amdgcn_s_memtime();
            const u32 v = __builtin_amdgcn_raw_buffer_load_b32(c.r, 0, o + k * 8192 + r * 256, 16);
            asm volatile("s_waitcnt vmcnt(0)" :: "v"(v) : "memory");
            const u32 d = (u32)__builtin_amdgcn_s_memtime() - t0;
            best[k] = d < best[k] ? d : best[k];
        }
    }
    __builtin_amdgcn_raw_buffer_store_b32(opq(best[1] < best[0] ? 1u : 0u), c.r, 0, INIT_OFF + pg * 4, 16);
}
DI void get_mask(Ctx& c) {
    const int l = lane_id();
    const u32 vo = (l < NPAGE ? l : 0) * 4;
    for (;;) {
        asm volatile("" ::: "memory");
        const u32 v = __builtin_amdgcn_raw_buffer_load_b32(c.r, vo, INIT_OFF, 16);
        if (__builtin_amdgcn_ballot_w64(v == SENT) == 0 || ++c.spin > SPIN_MAX) { c.par ^= (u32)__builtin_amdgcn_ballot_w64(v == 1u); return; }
    }
}

template <int N, int ST, int I = 0> DI void dma_x(const unsigned char* b, u32 voff, u32 dst) {
    if constexpr (I < N) { dmai<I * ST * 64, I * ST * 2048>(b, voff, dst); dma_x<N, ST, I + 1>(b, voff, dst); }
}
template <int N, int ST> DI void load_x(const Ctx& c, int s, int ks, int m, u32 dst) {
    const int l = lane_id();
    dma_x<N, ST>((const unsigned char*)c.x + (s * 40960 + m * 20480 + ks * 64), (l >> 2) * 1280 + (((l & 3) ^ (-(l >> 4) & 3)) << 4), dst + ks * 2048 + m * 1024);
}
DI u32 xfrag(int l) { return (l & 15) * 64 + (((l >> 4) ^ (-(l >> 2) & 3)) << 4); }
constexpr u32 V_XH = V_X - 16 * 2048, V_XL = V_X + 4 * 2048, L_XH = L_X - 16 * 2048, L_XL = L_X + 4 * 2048;
DI void prefetch_x(const Ctx& c, int s, u32 dump, int part = 0) {
    const unsigned char* b = (const unsigned char*)c.x + (s * 40960 + (c.wave + part) * 5120);
    const u32 voff = lane_id() * 16;
    dmai<0, 0>(b, voff, dump); dmai<1024, 0>(b, voff, dump); dmai<2048, 0>(b, voff, dump); dmai<3072, 0>(b, voff, dump); dmai<0, 0>(b + 4096, voff, dump);
}
DI u32 flag_load(const Ctx& c, int s, bool vm) {
    const int l = lane_id();
    const bool node = vm && c.h < 20;
    const u32 w = node ? (c.h & 1) * 2 : (c.h & 3), d = node ? 128 : 512 - w * 128;
    return __builtin_amdgcn_raw_buffer_load_b32(c.r, (l & 3) * 4 + ((l & 4) ? d : 0) + (node ? (l & 8) * 8 : 0), fb(c, s < 0 ? 0 : s) + w * 128 + (node ? c.wave & 3 : c.wave) * 16, 16);
}
DI u32 pending(u32 v) { u32 r = (u32)__builtin_amdgcn_ballot_w64(v == SENT); asm volatile("" : "+s"(r)); return r; }
DI void flag_wait(Ctx& c, int s, bool vm, u32 miss) {
    while (miss != 0 && ++c.spin < SPIN_MAX) { asm volatile("" ::: "memory"); miss = pending(flag_load(c, s, vm)); }
    asm volatile("" ::: "memory");
}

DI u32 mem_in(const Ctx& c, int s) { return mpb(c, s) + io((c.wave < 4 ? c.wave : 4) * 2048); }
template <u32 LM> DI void stage_mem(Ctx& c, int s, u32 o) {
    if (c.wave < 4 || c.wave == (LM == L_MEM ? 4 : 7)) {
        const int l = lane_id(), sl = c.wave < 4 ? c.wave : 4;
        v4u v[2];
        if (s == 0) {
            const __amdgpu_buffer_rsrc_t mr = __builtin_amdgcn_make_buffer_rsrc((void*)c.mem0, 0, 10240, 0x00020000);
            const u32 m = (l & 7) * 1280 + (l >> 4) * 32 + (l & 8) / 2;
#pragma unroll
            for (int k = 0; k < 2; ++k)
#pragma unroll
                for (int j = 0; j < 4; ++j) v[k][j] = __builtin_amdgcn_raw_buffer_load_b32(mr, m + k * 128 + j * 8, sl * 256, 0);
        } else {
            const u32 off[2] = {o, o}; const u32 voff[2] = {(u32)l * 16, (u32)l * 16 + 1024};
            poll<2>(c, off, voff, v);
        }
        st128(LM + sl * 2048 + l * 16, v[0]);
        st128(LM + sl * 2048 + 1024 + l * 16, v[1]);
    }
    if (LM == L_MEM) barv(); else bar();
}

struct NodeA { u32 u0, u1, px, pm, z, mo, mi, zo; };
DI NodeA node_addr(const Ctx& c, int t) {
    const u32 n = c.h, uo = (n >> 1) * 65536 + io(c.wave * 4096), po = mpb(c, t);
    NodeA a = {ub(c, t, (2 * n) & 3) + uo, ub(c, t, (2 * n + 1) & 3) + uo, po + MP_PX + io(c.wave * 2048), po + MP_PM + c.wave * 512,
               zb(c, t) + n * 32768 + c.wave * 1024, mpb(c, t + 1) + io((2 * n + (c.wave & 1)) * 256),
               po + io((c.wave < 4 ? c.wave : 4) * 2048), zb(c, t) + (c.wave & 3) * 16384 + n * 256};
    asm volatile("" : "+s"(a.u0), "+s"(a.u1), "+s"(a.px), "+s"(a.pm), "+s"(a.z), "+s"(a.mo), "+s"(a.mi), "+s"(a.zo));
    return a;
}
template <int I0, int I1> DI void node_u(const Ctx& c, const NodeA& na, v4u (&u)[2][4]) {
    const u32 l16 = lane_id() * 16 + (c.h < 20 ? 0u : 0x80000000u);
#pragma unroll
    for (int i = I0; i < I1; ++i) u[i >> 2][i & 3] = gld(c, i >> 2 ? na.u1 : na.u0, l16 + (i & 3) * 1024);
}
template <int SLP> DI void node_duty(Ctx& c, const NodeA& na, v4u (&u)[2][4]) {
    const int l = lane_id();
    const u32 l16 = l * 16;
    const u32 mo = na.mo;
    const u32 off[3] = {na.px, na.px, na.pm}, zoff[2] = {na.z, na.z + 16384};
    const u32 voff[3] = {l16, l16 + 1024, (u32)((l >> 4) * 128 + (l & 7) * 16)}, zvoff[2] = {l16, l16};
    v4u v[3], zz[2];
    const int jj = l & 3, r16 = (l >> 2) ^ ((jj >> 1) << 3);
    const u32 a = opq(V_MEM + (jj * 16 + ((c.wave ^ jj) & 1) * 8 + (r16 & 7)) * 16 + (r16 >> 3) * 8);
    const u32 ra = opq(V_MEM + c.wave * 1024 + (l16 ^ ((l & 16) << 3))), l4 = opq(r16 * 16 + jj * 4);
    __builtin_amdgcn_s_setprio(3);
    __builtin_amdgcn_s_sleep(SLP);
    TS(c, 5);
    zz[0] = gld(c, zoff[0], l16); zz[1] = gld(c, zoff[1], l16);
    __builtin_amdgcn_s_sleep(PSLEEP);
    poll<3>(c, off, voff, v);
    if (__builtin_amdgcn_ballot_w64(umax(max4(zz[0]), max4(zz[1])) == SENT) != 0) { ++c.spin; poll<2>(c, zoff, zvoff, zz); }
    TS(c, 6);
    const f4 z = {0, 0, 0, 0};
    f4 lo1 = mfma(u[1][0], v[0], z), hi0 = mfma(u[0][1], v[0], z);
    lo1 = mfma(u[1][2], v[1], lo1); hi0 = mfma(u[0][3], v[1], hi0);
    lo1 = mfma(zz[1], v[2], lo1);
    f4 lo0 = mfma(u[0][0], v[0], z), hi1 = mfma(u[1][1], v[0], z);
    SB();
    f4 r, w;
    lo0 = mfma(u[0][2], v[1], lo0);
#pragma unroll
    for (int i = 0; i < 4; ++i) w[i] = opqf((l & 8) ? hi0[i] : lo1[i]);
    SB();
    hi1 = mfma(u[1][3], v[1], hi1);
    lo0 = mfma(zz[0], v[2], lo0);
    SB();
#pragma unroll
    for (int i = 0; i < 4; ++i) r[i] = opqf(((l & 8) ? hi1[i] : lo0[i]) + ror8(w[i]));
    st128(ra, __builtin_bit_cast(v4u, r));
    bar();
    if (c.wave < 2) {
        f2 q[8];
#pragma unroll
        for (int w = 0; w < 8; ++w) q[w] = lds64f(a + w * 1024);
        const f2 s = ((q[0] + q[1]) + (q[2] + q[3])) + ((q[4] + q[5]) + (q[6] + q[7]));
        gst32(c, mo, l4, pkc(s.x, s.y));
    } else __builtin_amdgcn_s_sleep(BSLEEP);
    __builtin_amdgcn_s_setprio(0);
    TS(c, 7);
}

template <class F> DI void out_part(Ctx& c, int t, int j, f4 (&a)[2], F mid) {
    const u32 l16 = lane_id() * 16;
    a[0] = f4{0, 0, 0, 0}; a[1] = f4{0, 0, 0, 0};
    v4u u[4], px[4][2], z[3];
#pragma unroll
    for (int k = 0; k < 4; ++k) {
        u[k] = gld(c, ub(c, t, j & 3) + (j >> 2) * 65536 + io(c.wave * 4096), l16 + k * 1024);
        px[k][0] = gld(c, pxb(c, t) + c.wave * 16384 + (k >> 1) * 4096, l16 + (k & 1) * 2048);
        px[k][1] = gld(c, pxb(c, t) + c.wave * 16384 + (k >> 1) * 4096, l16 + (k & 1) * 2048 + 1024);
    }
    const u32 po = pxb(c, t) + 131072 + io(c.wave * 2048);
    const u32 off[1] = {zb(c, t) + j * 16384 + c.wave * 1024}; const u32 voff[1] = {l16};
    SB();
    mid();
    SB();
    a[0] = mfma(u[0], px[0][0], a[0]); a[1] = mfma(u[0], px[0][1], a[1]);
    SB();
    z[0] = gld(c, off[0], l16); z[1] = gld(c, po, l16); z[2] = gld(c, po, l16 + 1024);
    SB();
#pragma unroll
    for (int k = 1; k < 4; ++k) { a[0] = mfma(u[k], px[k][0], a[0]); a[1] = mfma(u[k], px[k][1], a[1]); }
    SB();
    if (__builtin_amdgcn_ballot_w64(max4(z[0]) == SENT) != 0) { v4u zz[1]; poll<1>(c, off, voff, zz); z[0] = zz[0]; }
    a[0] = mfma(z[0], z[1], a[0]); a[1] = mfma(z[0], z[2], a[1]);
}
DI void out_store(const Ctx& c, int t, int j, int m, f4 s) {
    const int l = lane_id();
    v2u o = {pk(s[0], s[1]), pk(s[2], s[3])};
    const __amdgpu_buffer_rsrc_t orr = __builtin_amdgcn_make_buffer_rsrc((void*)c.out, 0, 2048 * 2560 - 1280, 0x00020000);
    __builtin_amdgcn_raw_buffer_store_b64(o, orr, (l & 15) * 2560 + (l >> 4) * 8, (t * 32 + m * 16) * 2560 + j * 32, 0);
}
template <u32 LR, class F> DI void out_duty(Ctx& c, int t, int j, F mid) {
    f4 a[2];
    out_part(c, t, j, a, [] {});
    const u32 l16 = lane_id() * 16;
    mid();
    if (c.wave != 0) st128(LR + (c.wave - 1) * 1024 + l16, __builtin_bit_cast(v4u, a[0]));
    if (c.wave != 1) st128(LR + 7168 + (c.wave ? c.wave - 1 : 0) * 1024 + l16, __builtin_bit_cast(v4u, a[1]));
    bar();
    if (c.wave < 2) {
        f4 s = c.wave ? a[1] : a[0];
#pragma unroll
        for (int w = 0; w < 7; ++w) s += __builtin_bit_cast(f4, lds128(LR + c.wave * 7168 + w * 1024 + l16));
        out_store(c, t, j, c.wave, s);
    }
    bar();
}

template <bool WO> DI TB wbase(const Ctx& c, int l) {
    return mktb(WO ? 12288 + (l >> 4) * 1024 + (c.wave & 3) * 256 + rowp(l) : (c.wave >> 2) * 12288 + (l >> 4) * 3072 + (c.wave & 3) * 768 + rowp(l));
}
template <bool WO> DI void issue_chunk(const unsigned char* wb, u32 voff, int wave, int ch) {
    const u32 slot = ((ch + 2) % 6) * 24576;
#pragma unroll
    for (int u = 0; u < 3; ++u) {
        if (WO) dma(wb + (size_t)(3 * (ch & 1) + u) * 40960 + (ch >> 1) * 128, voff, slot + 12288 + (u * 4 + (wave & 3)) * 1024);
        else dma(wb + (size_t)ch * 393216 + u * 128, voff, slot + (wave >> 2) * 12288 + (wave & 3) * 3072 + u * 1024);
    }
}
template <bool WO> DI void load_weights(const Ctx& c, const u16* w, v8u (&W)[24]) {
    const int l = lane_id();
    const int kb = c.wave & 3, rr = ((l >> 1) & 7) ^ (4 * (kb & 1));
    const u32 voff = (8 * kb + rr) * (WO ? 1280 : 12288) + (l >> 4) * 32 + (l & 1) * 16;
    const unsigned char* wb = (const unsigned char*)w + (WO ? (size_t)c.h * 245760 : (size_t)c.h * 384);
    const TB wl = wbase<WO>(c, l);
    const size_t half = HALFSW * c.p * (WO ? (size_t)512 : (size_t)8 * 393216);
#pragma unroll
    for (int i = 0; i < 5; ++i) issue_chunk<WO>(wb + half, voff, c.wave, i);
#pragma unroll
    for (int i = 0; i < 16; ++i) {
        asm volatile("s_waitcnt vmcnt(12) lgkmcnt(0)\n\ts_barrier" ::: "memory");
        if (i < 15) issue_chunk<WO>(i + 5 < 8 ? wb + half : (i + 5 < 16 ? wb - half : wb), voff, c.wave, i + 5);
        const u32 slot = ((i + 2) % 6) * 24576;
#pragma unroll
        for (int u = 0; u < 3; ++u) {
            v4u f = trfrag(wl, slot + u * (WO ? 4096 : 256));
            const int ks = 3 * (i & 1) + u;
            const int pi = WO ? (i >> 1) * 3 + (ks >> 1) : u * 8 + (i >> 1), hf = WO ? (ks & 1) : (i & 1);
            W[pi] = hf ? cat8(lo4(W[pi]), f) : cat8(f, f);
        }
    }
    const u32 on = __builtin_amdgcn_readfirstlane(0u - (u32)c.p);
#pragma unroll
    for (int i = 0; i < 12 * HALFSW; ++i) {
        const int a = WO ? i : (i >> 2) * 8 + (i & 3), b = WO ? i + 12 : a + 4;
#pragma unroll
        for (int k = 0; k < 8; k += 2) {
            u32 x = W[a][k], y = W[b][k], z = W[a][k + 1], t = W[b][k + 1];
            asm volatile("s_mov_b32 exec_lo, %4\n\ts_mov_b32 exec_hi, %4\n\tv_swap_b32 %0, %1\n\tv_swap_b32 %2, %3\n\ts_mov_b64 exec, -1" : "+v"(x), "+v"(y), "+v"(z), "+v"(t) : "s"(on));
            W[a][k] = x; W[b][k] = y; W[a][k + 1] = z; W[b][k + 1] = t;
        }
    }
    barv();
}

template <u32 LM, class F> DI u32 crit_gemm1(Ctx& c, int s, const v8u (&W)[24], int l, u32 d01, u32 d2, u32 mi, F late) {
    TB wl = wbase<false>(c, l);
    v8u T[3];
#pragma unroll
    for (int u = 0; u < 3; ++u) T[u] = cat8(trfrag(wl, u * 256), trfrag(wl, 24576 + u * 256));
    wl.a = opq(wl.a + 49152); wl.b = opq(wl.b + 49152);
    const u32 idx = opq(0x4444u + (l & 8) * 0x1555u + ((l & 8) >> 2) + (LM == L_MEM ? ((l >> 4) * 256 + rowp(l)) << 16 : 0u)), ab = opq(LM + l * 16);
    d01 = opq(d01); d2 = opq(d2);
    f4 acc[3] = {{0, 0, 0, 0}, {0, 0, 0, 0}, {0, 0, 0, 0}};
    asm volatile("" : "+v"(acc[0]), "+v"(acc[1]), "+v"(acc[2]));
    TS(c, 0);
    stage_mem<LM>(c, s, mi);
    TS(c, 1);
    v4u a[11];
    a[0] = lds128(ab + 8192);
    SB();
#pragma unroll
    for (int i = 1; i < 4; ++i) a[i] = lds128(ab + (i - 1) * 1024);
    SB();
#pragma unroll
    for (int i = 0; i < 10; ++i) {
#pragma unroll
        for (int u = 0; u < 3; ++u) acc[u] = smf(a[i], (i == 0 || i == 9) ? T[u] : W[u * 8 + i - 1], acc[u], idx);
        SB();
        if (i + 4 < 10) a[i + 4] = lds128(ab + (i + 4 == 9 ? 9 : i + 3) * 1024);
        if (i >= 3 && i < 6) T[i - 3] = cat8(trfrag(wl, (i - 3) * 256), trfrag(wl, 24576 + (i - 3) * 256));
        late(i);
        SB();
        if (LM == L_MEM && i == 0) TS2(c, 5);
    }
    if (LM == L_MEM) TS2(c, 6);
    float r[4];
#pragma unroll
    for (int i = 0; i < 4; ++i) r[i] = fold32(acc[0][i], acc[1][i]);
    st64(d01, v2u{pk(r[0], r[1]), pk(r[2], r[3])});
    st32(d2, pk(fold32(acc[2][0], acc[2][2]), fold32(acc[2][1], acc[2][3])));
    if (LM == L_MEM) TS2(c, 7);
    SB();
    return idx >> 16;
}

DI constexpr int step_ks(int q) { return q < 8 ? ((q & 1) ? 16 + (q >> 1) : (q >> 1)) : q - 4; }
DI constexpr u32 step_slot(int q) { return (step_ks(q) >= 16 ? step_ks(q) - 16 : step_ks(q) + 4) * 2048; }
template <bool VM, class F> DI void bulk_gemm1(Ctx& c, const v8u (&W)[24], int s, F hook) {
    hook(-1);
    const int l = lane_id();
    const TB wl = wbase<false>(c, l);
    const u32 ab = opq((VM ? V_X : L_X) + xfrag(l));
    f4 acc[2][3];
#pragma unroll
    for (int m = 0; m < 2; ++m)
#pragma unroll
        for (int u = 0; u < 3; ++u) acc[m][u] = f4{0, 0, 0, 0};
    v4u a[22][2], b[5][3];
#pragma unroll
    for (int i = 0; i < 4; ++i) a[i >> 1][i & 1] = lds128(ab + step_slot(i >> 1) + (i & 1) * 1024);
#pragma unroll
    for (int u = 0; u < 3; ++u) b[0][u] = trfrag(wl, u * 256);
#pragma unroll
    for (int q = 0; q < 20; ++q) {
        const int ks = step_ks(q);
        if (VM && q == 10) {
            const int l2 = lane_id();
            const unsigned char* g = (const unsigned char*)c.x + (size_t)s * 40960 + 960;
            const u32 vo = (l2 & 15) * 1280 + (l2 >> 4) * 16;
            a[19][0] = *(const v4u*)(g + vo); a[19][1] = *(const v4u*)(g + 20480 + vo);
        }
        hook(q);
        SB();
#pragma unroll
        for (int m = 0; m < 2; ++m) {
#pragma unroll
            for (int u = 0; u < 3; ++u) {
                acc[m][u] = mfma(a[q][m], ks >= 16 ? b[ks - 16][u] : ((ks & 1) ? hi4(W[u * 8 + (ks >> 1)]) : lo4(W[u * 8 + (ks >> 1)])), acc[m][u]);
                if (m == 1 && ks >= 16 && ks < 19) { SB(); b[ks - 15][u] = trfrag(wl, (ks - 15) * 24576 + u * 256); SB(); }
            }
            SB();
            if (q < (VM ? 17 : 18)) a[q + 2][m] = lds128(ab + step_slot(q + 2) + m * 1024);
            SB();
        }
    }
    const int l3 = lane_id(), cc = l3 & 15;
    const u32 ia = L_IMG + (c.wave >> 2) * 12288 + ((c.wave & 3) * 48 + (cc ^ (4 * (cc >> 3)))) * 32 + (l3 >> 4) * 8;
    v2u v[2][3];
#pragma unroll
    for (int m = 0; m < 2; ++m)
#pragma unroll
        for (int u = 0; u < 3; ++u) v[m][u] = v2u{pk(acc[m][u][0], acc[m][u][1]), pk(acc[m][u][2], acc[m][u][3])};
    hook(20);
#pragma unroll
    for (int m = 0; m < 2; ++m)
#pragma unroll
        for (int u = 0; u < 3; ++u) st64(ia + m * 6144 + u * 512, v[m][u]);
}

DI bool out_vm(const Ctx& c) { return c.h >= 20 && c.h < 28; }
DI void vm_duty(Ctx& c, int s) {
    if (s + 6 < NSEG && c.wave >= 4 && c.wave < 7) {
        load_x<2, 2>(c, s + 6, 16 + ((c.wave >> 1) & 1), c.wave & 1, V_XH);
        load_x<7, 2>(c, s + 6, (c.wave >> 1) & 1, c.wave & 1, V_XL);
        if (c.wave < 6) { load_x<1, 1>(c, s + 6, 14, c.wave & 1, V_XL); load_x<3, 2>(c, s + 6, 1 + (c.wave & 1) * 6, 1, V_XL); }
        else { load_x<2, 2>(c, s + 6, 17, 1, V_XH); load_x<1, 1>(c, s + 6, 13, 1, V_XL); }
    }
    if (out_vm(c) && s >= 0) {
        flag_wait(c, s, true, 1);
        if (s + 2 >= NSEG) out_duty<V_MEM>(c, s, c.h + 12, [] {});
    }
}

template <bool MEM> DI void attn8(Ctx& c, int s, u32 pa) {
    v4u k[6][2], q[6], m[6];
    constexpr int PF = MEM ? MPF : 3;
    if (!MEM) bar();
    const TB il = mktb(L_IMG + 12288 + pa), fl = mktb(L_F + (pa & ~16u));
#pragma unroll
    for (int ks = 0; ks < (MEM ? MPRE : PF); ++ks) { k[ks][0] = trfrag(il, ks * 1024); k[ks][1] = trfrag(il, 6144 + ks * 1024); }
    f4 t[3] = {{0, 0, 0, 0}, {0, 0, 0, 0}, {0, 0, 0, 0}};
    if (MEM) {
        SB();
        asm volatile("s_waitcnt lgkmcnt(%0)\n\ts_barrier" :: "n"(4 * MPRE) : "memory");
#pragma unroll
        for (int ks = 0; ks < 3; ++ks) {
            q[ks] = trfrag(fl, ks * 1024);
            if (ks >= MPRE && ks < PF) { k[ks][0] = trfrag(il, ks * 1024); k[ks][1] = trfrag(il, 6144 + ks * 1024); }
            SB();
        }
        SB();
    }
    const int l = lane_id(), g = l >> 4, cc = l & 15;
    const u32 qo = L_IMG + 6144 - L_F + (c.wave & 1) * 16;
    const TB ql = TB{fl.a + qo, fl.b + qo};
    const u32 po = MEM ? mpb(c, s) : pxb(c, s) + (c.wave & 1) * 128;
    const u32 o1 = MEM ? po + MP_PX + io((c.h >> 1) * 1024) + (c.h & 1) * 128 : po + io(c.h * 2048 + 1024);
    const u32 o2 = MEM ? po + MP_PM + c.h * 128 : po + 131072 + io((c.h >> 2) * 2048 + 1024) + (c.h & 3) * 256;
    const u32 v1 = opq(g * 256 + (cc & 7) * 16 + (cc & 8)), v2 = opq(cc < 8 ? cc * 16 + (g & 1) * 8 + (g >> 1) * 4 : 0x80000000u);
    asm volatile("" :: "s"(o1), "s"(o2));
    if (MEM) {
        __builtin_amdgcn_s_setprio(0);
        SB();
#pragma unroll
        for (int ks = 0; ks < 6; ++ks) {
            t[1] = mfma(k[ks][0], q[ks], t[1]);
            t[2] = mfma(k[ks][1], q[ks], t[2]);
            SB();
            if (ks + PF < 6) { k[ks + PF][0] = trfrag(il, (ks + PF) * 1024); k[ks + PF][1] = trfrag(il, 6144 + (ks + PF) * 1024); }
            if (ks < 3) q[ks + 3] = trfrag(fl, (ks + 3) * 1024);
            SB();
        }
        __builtin_amdgcn_s_setprio(3);
        bar();
        TS(c, 2);
#pragma unroll
        for (int ks = 0; ks < 6; ++ks) { m[ks] = trfrag(fl, ks * 1024 + 16); SB(); }
    }
    float v[5], mx;
    if (!MEM) {
        bar();
        TS(c, 2);
        q[0] = trfrag(ql, 0); q[1] = trfrag(ql, 1024); m[0] = trfrag(fl, 16);
        SB();
#pragma unroll
        for (int ks = 0; ks < 6; ++ks) {
            t[1] = mfma(k[ks][0], q[ks], t[1]);
            SB();
            if (ks < 5) m[ks + 1] = trfrag(fl, (ks + 1) * 1024 + 16);
            SB();
            t[2] = mfma(k[ks][1], q[ks], t[2]);
            SB();
            if (ks + PF < 6) { k[ks + PF][0] = trfrag(il, (ks + PF) * 1024); k[ks + PF][1] = trfrag(il, 6144 + (ks + PF) * 1024); }
            SB();
            t[0] = mfma(m[ks], q[ks], t[0]);
            SB();
            if (ks < 4) q[ks + 2] = trfrag(ql, (ks + 2) * 1024);
            SB();
        }
    }
    const float sc = 0.07216878364870322f * 1.4426950408889634f;
#pragma unroll
    for (int i = 0; i < 4; ++i) {
        v[i] = cc < 8 ? t[1][i] : t[2][i];
        if (!MEM) v[i] = (4 * g + i + 8 > (c.wave & 1) * 8 + cc && cc >= 8) ? __uint_as_float(opq(0xFF800000u)) : v[i];
    }
    mx = opqf(fmx(fmx(v[0], v[1]), fmx(v[2], v[3])));
    if (MEM) {
        SB();
#pragma unroll
        for (int ks = 0; ks < 6; ++ks) t[0] = mfma(m[ks], q[ks], t[0]);
        SB();
    }
    v[4] = g < 2 ? (cc < 8 ? t[0][0] : t[0][1]) : (cc < 8 ? t[0][2] : t[0][3]);
    mx = fmx(mx, v[4]);
    mx = xmax(fmx(mx, ror8(mx))) * sc;
#pragma unroll
    for (int i = 0; i < 5; ++i) v[i] = __builtin_amdgcn_exp2f(__builtin_fmaf(v[i], sc, -mx));
    float sum = (v[0] + v[1]) + (v[2] + v[3]) + v[4];
    sum = xsum(sum + ror8(sum));
    const float inv = __builtin_amdgcn_rcpf(sum);
    gst64(c, o1, v1, v2u{pkc(v[0] * inv, v[1] * inv), pkc(v[2] * inv, v[3] * inv)});
    const float w = v[4] * inv;
    gst32(c, o2, v2, pkc(w, ror8(w)));
    __builtin_amdgcn_s_setprio(0);
}

DI void attn16(Ctx& c, int s) {
    bar();
    const int l = lane_id(), g = l >> 4, cc = l & 15;
    const TB il = mktb(L_IMG + g * 256 + rowp(l));
    f4 t[2] = {{0, 0, 0, 0}, {0, 0, 0, 0}};
    v4u q[6], k[6], m[6];
#pragma unroll
    for (int ks = 0; ks < 4; ++ks) { q[ks] = trfrag(il, ks * 1024); k[ks] = trfrag(il, 12288 + ks * 1024); }
    bar();
    TS(c, 2);
    m[0] = trfrag(il, L_F - L_IMG); m[1] = trfrag(il, L_F - L_IMG + 1024);
    SB();
#pragma unroll
    for (int ks = 0; ks < 6; ++ks) {
        t[1] = mfma(k[ks], q[ks], t[1]); t[0] = mfma(m[ks], q[ks], t[0]);
        SB();
        if (ks < 4) m[ks + 2] = trfrag(il, L_F - L_IMG + (ks + 2) * 1024);
        if (ks < 2) { q[ks + 4] = trfrag(il, (ks + 4) * 1024); k[ks + 4] = trfrag(il, 12288 + (ks + 4) * 1024); }
        SB();
    }
    const float sc = 0.07216878364870322f * 1.4426950408889634f, ninf = __uint_as_float(opq(0xFF800000u));
    float mx = ninf;
#pragma unroll
    for (int k = 0; k < 2; ++k)
#pragma unroll
        for (int i = 0; i < 4; ++i) {
            const bool vis = k == 0 ? g >= 2 : (4 * g + i <= cc);
            t[k][i] = vis ? t[k][i] : ninf; mx = fmx(mx, t[k][i]);
        }
    mx = xmax(mx) * sc;
    float sum = 0;
#pragma unroll
    for (int k = 0; k < 2; ++k)
#pragma unroll
        for (int i = 0; i < 4; ++i) { t[k][i] = __builtin_amdgcn_exp2f(__builtin_fmaf(t[k][i], sc, -mx)); sum += t[k][i]; }
    sum = xsum(sum);
    const float inv = __builtin_amdgcn_rcpf(sum);
    const v4u px = {pk(t[1][0] * inv, t[1][1] * inv), pk(t[1][2] * inv, t[1][3] * inv), 0u, 0u};
    const v2u pm = {pk(t[0][0] * inv, t[0][1] * inv), pk(t[0][2] * inv, t[0][3] * inv)};
    const u32 po = pxb(c, s);
    gst(c, po + io(c.h * 2048), l * 16, px);
    if (g >= 2) gst64(c, po + 131072 + io((c.h >> 2) * 2048) + (c.h & 3) * 256, cc * 16 + (g & 1) * 8, pm);
    __builtin_amdgcn_s_setprio(0);
}
DI void load_x_main(const Ctx& c, int s) {
    const int pw = (c.wave >> 1) ^ 2;
    load_x<1, 1>(c, s, 16 + pw, c.wave & 1, L_XH);
    load_x<2, 1>(c, s, 2 * pw, c.wave & 1, L_XL);
}

DI void qk_main(Ctx& c, const u16* wq, const u16* wk) {
    v8u W[24];
    if (c.par == 0 && c.p == 1 && c.h < NPAGE && c.wave == 0) probe_prep(c, c.h);
    load_x<2, 4>(c, c.p, 8 + (c.wave >> 1), c.wave & 1, L_XL);
    load_weights<false>(c, c.wave < 4 ? wq : wk, W);
    if (c.par == 0 && c.p == 1 && c.h < NPAGE) { if (c.wave == 0) probe_page(c, c.h); bar(); }
    load_x_main(c, c.p);
    barv();
    for (int s = c.p - 2; s < NSEG; s += 2) {
        asm volatile("" : "+s"(c.wave), "+s"(c.h), "+s"(c.par));
        if (s >= 0) {
            const int l = lane_id(), g = l >> 4, cc = l & 15;
            const u32 fa = L_F + ((c.wave & 3) * 48 + (cc ^ (4 * (cc >> 3)))) * 32 + (c.wave >> 2) * 16 + (g & 1) * 8;
            if (c.wave == 0) __builtin_amdgcn_s_setprio(3);
            else if (c.wave < 4) __builtin_amdgcn_s_setprio(2);
            else __builtin_amdgcn_s_setprio(1);
            const u32 pa = crit_gemm1<L_MEM>(c, s, W, l, fa + (g >> 1) * 512, fa + 1024 + (g >> 1) * 4, mem_in(c, s), [&](int i) {
                if (i == QB) asm volatile("s_cmp_lt_u32 %0, 4\n\ts_cbranch_scc1 1f\n\ts_barrier\n1:" :: "s"(c.wave) : "memory", "scc");
            });
            if (c.wave == 0) attn8<true>(c, s, pa);
            else if (c.wave == 1) attn16(c, s);
            else if (c.wave < 4) attn8<false>(c, s, pa);
            else {
                bar();
                __builtin_amdgcn_s_setprio(0);
                TS(c, 2);
                if (c.wave >= 6 && s + 2 < NSEG) load_x<5, 1>(c, s + 2, 8, c.wave & 1, L_XL);
                if (c.wave == 4) __builtin_amdgcn_s_sleep(QSLEEP);
            }
            TS(c, 3);
            if (s < 2) {
                const v4u sv = sentv();
                for (int i = c.wave * 64 + lane_id(); i < 30 * 40; i += 512) {
                    const int st = c.p + 4 + 2 * (i / 40), e = i % 40;
                    const u32 o = e < 32 ? MP_PX + io((c.h >> 1) * 1024) + ((e >> 3) * 16 + (c.h & 1) * 8 + (e & 7)) * 16 : MP_PM + c.h * 128 + (e & 7) * 16;
                    gst(c, 0, mpb(c, st) + o, sv);
                }
            }
        }
        auto flag_p = [&] { gst32(c, fb(c, s < 0 ? 0 : s) + 512 + c.h * 4, c.wave == 7 && s >= 0 ? 0u : 0x80000000u, opq(1u)); };
        if (s + 2 < NSEG) {
            f4 a[2];
            if (c.wave < 4) {
                bar();
                TS1(c, 5);
                if (c.wave < 2 && s >= 0) load_x<3, 1>(c, s + 2, 13, c.wave & 1, L_XL);
                if (s >= 2) {
                    out_part(c, s - 2, c.h, a, [] { asm volatile("s_waitcnt vmcnt(12)\n\ts_barrier" ::: "memory"); });
                    const u32 pa = L_IMG + c.wave * 2048 + lane_id() * 16;
                    st128(pa, __builtin_bit_cast(v4u, a[0])); st128(pa + 1024, __builtin_bit_cast(v4u, a[1]));
                } else barv();
                TS1(c, 6);
            }
            bulk_gemm1<false>(c, W, s + 2, [&](int q) {
                if (q == QM1) bar_half<true, true>(c.wave);
                if (q == QM0) bar_half<false, true>(c.wave);
                if (q == 15) bar_half<false, false>(c.wave);
                if (q == 20) { TS1(c, 7); bar_half<true, false>(c.wave); }
            });
            if (c.wave >= 4) {
                TS1(c, 5);
                if (s >= 2) {
                    out_part(c, s - 2, c.h, a, [&] { asm volatile("s_barrier" ::: "memory"); flag_p(); });
                    const u32 l16 = lane_id() * 16, pa = L_IMG + (c.wave - 4) * 2048 + l16;
                    a[0] += __builtin_bit_cast(f4, lds128(pa)); a[1] += __builtin_bit_cast(f4, lds128(pa + 1024));
                    const u32 pb = L_X + (c.wave >= 6 ? c.wave - 2 : 0) * 2048 + l16;
                    if (c.wave != 4) st128(pb, __builtin_bit_cast(v4u, a[0]));
                    if (c.wave != 5) st128(pb + 1024, __builtin_bit_cast(v4u, a[1]));
                    TS1(c, 6);
                    bar();
                    if (c.wave < 6) {
                        const u32 pc = L_X + (c.wave & 1) * 1024 + l16;
                        f4 r = (c.wave & 1) ? a[1] : a[0];
                        r += __builtin_bit_cast(f4, lds128(pc));
                        r += __builtin_bit_cast(f4, lds128(pc + 8192)) + __builtin_bit_cast(f4, lds128(pc + 10240));
                        out_store(c, s - 2, c.h, c.wave & 1, r);
                    }
                } else { bar(); flag_p(); bar(); }
            }
            TS(c, 4);
            if (s + 4 < NSEG) load_x_main(c, s + 4);
            if (s < 0) get_mask(c);
            else flag_wait(c, s, false, 1);
        } else {
            barv(); flag_p();
            for (int t = s - 2; t <= s; t += 2) { flag_wait(c, t, false, 1); out_duty<L_MEM>(c, t, c.h, [] {}); }
        }
        TSF(c, s);
    }
}

DI u32 wo_off(int i, int ks) { return ((i - 8) * 2 + ks / 3) * 24576 + (ks % 3) * 4096; }
DI u32 z_lane(int l) { const int g = l >> 4; return ((g >> 1) + 2 * (g & 1)) * 65536 + (l & 15) * 16; }
DI void swap32(f4& a, f4& b, int i) {
    auto s = __builtin_amdgcn_permlane32_swap(__float_as_uint(a[i]), __float_as_uint(b[i]), false, false);
    a[i] = __uint_as_float(s[0]); b[i] = __uint_as_float(s[1]);
}
DI v4u z_pack(const u32 (&x)[2], const u32 (&y)[2]) {
    auto s0 = __builtin_amdgcn_permlane16_swap(x[0], y[0], false, false);
    auto s1 = __builtin_amdgcn_permlane16_swap(x[1], y[1], false, false);
    return v4u{s0[0], s1[0], s0[1], s1[1]};
}
template <class F> DI void z_tail(Ctx& c, u32 zo, TB wl, TB wh, TB il, F mid) {
    v4u a[3]; v8u T[2][3];
    asm volatile("" ::: "memory");
    SB();
    T[0][0] = cat8(trfrag(wl, wo_off(8, 0)), trfrag(wl, wo_off(8, 1))); T[1][0] = cat8(trfrag(wh, wo_off(8, 0)), trfrag(wh, wo_off(8, 1)));
    asm volatile("s_waitcnt lgkmcnt(8)\n\ts_barrier" ::: "memory");
    TS(c, 2);
#pragma unroll
    for (int f = 0; f < 3; ++f) a[f] = trfrag(il, f * 1024);
    SB();
    const int l = lane_id(), g = l >> 4;
    const u32 idx = 0x4444u + (l & 8) * 0x1555u + ((l & 8) >> 2);
    f4 acc[2] = {{0, 0, 0, 0}, {0, 0, 0, 0}};
    mid();
    SB();
#pragma unroll
    for (int f = 0; f < 3; ++f)
#pragma unroll
        for (int j = 0; j < 2; ++j) {
            acc[j] = smf(a[f], T[j][f], acc[j], idx);
            SB();
            if (f < 2) T[j][f + 1] = cat8(trfrag(j ? wh : wl, wo_off(8, 2 * f + 2)), trfrag(j ? wh : wl, wo_off(8, 2 * f + 3)));
            SB();
        }
    const u32 x[2] = {pkc(fold32(acc[0][0], acc[1][0]), fold32(acc[0][1], acc[1][1])), pkc(fold32(acc[0][2], acc[1][2]), fold32(acc[0][3], acc[1][3]))};
    gst(c, zo, (g & 1) ? 0x80000000u : z_lane(l), z_pack(x, x));
    __builtin_amdgcn_s_setprio(0);
}

DI void v_main(Ctx& c, const u16* wv) {
    v8u W[24];
    load_weights<false>(c, wv, W);
    NodeA na = {};
    for (int s = c.p - 4; s < NSEG; s += 2) {
        asm volatile("" : "+s"(c.wave), "+s"(c.h), "+s"(c.par));
        if (s >= 0) {
            const int l = lane_id(), g = l >> 4, cc = l & 15;
            const int rho = ((cc >> 2) << 1) | (cc & 1);
            const u32 a = V_VS + ((cc >> 1) & 1) * 16 + (g & 1) * 8;
            const int t0 = c.wave * 3 + (g >> 1), t2 = c.wave * 3 + 2;
            TB wl, wh, il;
            const u32 pa = opq((12288 + g * 1024 + c.wave * 256 + rowp(l)) | (g * 256 + rowp(l)) << 16);
            __builtin_amdgcn_s_setprio(VPRIO);
            crit_gemm1<V_MEM>(c, s, W, l, a + (t0 * 8 + (rho ^ (4 * (t0 & 1)))) * 32, a + (t2 * 8 + (rho ^ (4 * (t2 & 1)))) * 32 + (g >> 1) * 4,
                na.mi, [&](int i) {
                    if (i == 7) { wl = mktb(pa & 0xFFFFu); il.a = opq(V_VS + (pa >> 16)); }
                    if (i == 8) { il.b = opq(il.a ^ 128u); wh = TB{opq(wl.a + 49152), opq(wl.b + 49152)}; }
                });
            v4u u[2][4];
            z_tail(c, na.zo + 2 * 262144, wl, wh, il, [&] { node_u<0, VPRE>(c, na, u); });
            TS(c, 6);
            if (out_vm(c) && s >= 2) {
                f4 a[2];
                __builtin_amdgcn_s_sleep(OVSLEEP);
                out_part(c, s - 2, c.h + 12, a, [] {});
                const u32 pa = V_MEM + c.wave * 2048 + lane_id() * 16;
                st128(pa, __builtin_bit_cast(v4u, a[0])); st128(pa + 1024, __builtin_bit_cast(v4u, a[1]));
                TS(c, 5);
            } else if (c.h >= 20) { if (c.h >= 28 && s + 8 < NSEG) { prefetch_x(c, s + 8, V_DUMP); prefetch_x(c, s + 8, V_DUMP, 4); } }
            else if (s < NSEG - 1) { node_u<VPRE, 8>(c, na, u); node_duty<NSLEEP + NVX>(c, na, u); }
            if (c.h < 20 && s < 2) {
                const v4u sv = sentv();
                for (int i = c.wave * 64 + lane_id(); i < 30 * 32; i += 256) {
                    const int st = 5 + c.p + 2 * (i >> 5);
                    if (st < NSEG) gst(c, 0, mpb(c, st) + io(c.h * 512) + (i & 31) * 16, sv);
                }
            }
        } else {
            if (s >= -2) get_mask(c);
            TS(c, 6);
            barv();
            TS(c, 5);
        }
        if (s + 4 < NSEG) bulk_gemm1<true>(c, W, s + 4, [&](int q) {
            if (q == 9) na = node_addr(c, (s + 2) & 63);
            if (q == 20) { TS(c, 3); bar(); }
        });
        else { na = node_addr(c, (s + 2) & 63); bar(); }
        if (out_vm(c) && s >= 2) bar();
        TS(c, 4);
        vm_duty(c, s);
        TSF(c, s);
    }
}

DI void bulk_gemm2(const Ctx& c, const v8u (&W)[24], int s) {
    const int l = lane_id();
    const TB wl = wbase<true>(c, l), il = mktb(L_IMG + (l >> 4) * 256 + rowp(l));
    const u32 uo = ub(c, s, c.wave & 3) + io(c.h * 1024);
    v4u a[20][2], b[20];
#pragma unroll
    for (int i = 0; i < 4; ++i) a[i >> 1][i & 1] = trfrag(il, (i & 1) * 6144 + (i >> 1) * 1024);
#pragma unroll
    for (int ig = 0; ig < 3; ++ig) {
        const int nt = ig == 0 ? 4 : 3;
        f4 acc[2][4];
#pragma unroll
        for (int j = 0; j < 4; ++j) { acc[0][j] = f4{0, 0, 0, 0}; acc[1][j] = f4{0, 0, 0, 0}; }
#pragma unroll
        for (int ks = 0; ks < 6; ++ks) {
            const int st = ig * 6 + ks;
#pragma unroll
            for (int m = 0; m < 2; ++m) {
                SB();
#pragma unroll
                for (int j = 0; j < nt; ++j) {
                    const int i = ig == 0 ? j : (j < 2 ? 2 + 2 * ig + j : 7 + ig);
                    acc[m][j] = mfma(a[st][m], i < 8 ? ((ks & 1) ? hi4(W[i * 3 + (ks >> 1)]) : lo4(W[i * 3 + (ks >> 1)])) : b[st], acc[m][j]);
                    if (m == 0 && j == 0 && st >= 5 && st < 17) { SB(); b[st + 1] = trfrag(wl, wo_off(7 + (st + 1) / 6, (st + 1) % 6)); SB(); }
                }
                SB();
                if (st < 16) a[st + 2][m] = trfrag(il, m * 6144 + ((st + 2) % 6) * 1024);
            }
        }
        SB();
#pragma unroll
        for (int j = 0; j < nt; ++j) {
            const int i = ig == 0 ? j : (j < 2 ? 2 + 2 * ig + j : 7 + ig);
            const v4u v = {pk(acc[0][j][0], acc[0][j][1]), pk(acc[0][j][2], acc[0][j][3]), pk(acc[1][j][0], acc[1][j][1]), pk(acc[1][j][2], acc[1][j][3])};
            gst(c, uo + i * 65536, l * 16, v);
        }
        SB();
    }
}

DI void o_main(Ctx& c, const u16* wo) {
    v8u W[24];
    load_weights<true>(c, wo, W);
    load_x<2, 2>(c, c.p, 16 + ((c.wave >> 1) & 1), c.wave & 1, V_XH);
    load_x<4, 2>(c, c.p, (c.wave >> 1) & 1, c.wave & 1, V_XL);
    for (int s = c.p - 4; s < NSEG; s += 2) {
        asm volatile("" : "+s"(c.wave), "+s"(c.h), "+s"(c.par));
        if (s >= 0) {
            const int l = lane_id(), g = l >> 4;
            const TB il = mktb(V_VS + g * 256 + rowp(l));
            const u32 idx = opq(0x4444u + (l & 8) * 0x1555u + ((l & 8) >> 2)), za = opq(z_lane(l));
            const NodeA na = node_addr(c, s);
            u32 zo = na.zo, zo1 = zo + 262144;
            asm volatile("" : "+s"(zo1));
            stage_mem<V_MEM>(c, s, na.mi);
            v4u u[2][4];
            if (c.h < 20) flag_wait(c, s, true, 1);
            node_u<0, UPRE>(c, na, u);
            f4 acc[8];
#pragma unroll
            for (int i = 0; i < 4; ++i) { acc[i] = f4{0, 0, 0, 0}; asm volatile("" : "+v"(acc[i])); }
            __builtin_amdgcn_s_setprio(OPRIO);
            TS2(c, 6);
            asm volatile("s_waitcnt vmcnt(%0) lgkmcnt(0)\n\ts_barrier" :: "n"(UPRE) : "memory");
            TS(c, 2);
            v4u a[3];
#pragma unroll
            for (int f = 0; f < 3; ++f) a[f] = trfrag(il, f * 1024);
            SB();
            u32 xp[4][2];
#pragma unroll
            for (int pr = 0; pr < 5; ++pr) {
                float x[4];
#pragma unroll
                for (int n = 0; n < 6; ++n) {
                    if (pr < 4) acc[2 * pr + (n & 1)] = smf(a[n >> 1], W[(2 * pr + (n & 1)) * 3 + (n >> 1)], acc[2 * pr + (n & 1)], idx);
                    SB();
                    if (pr > 0) {
                        f4& p = acc[2 * pr - 2]; f4& q = acc[2 * pr - 1];
                        if (n == 1) { swap32(p, q, 0); swap32(p, q, 1); }
                        if (n == 2) { swap32(p, q, 2); swap32(p, q, 3); x[0] = opqf(p[0] + q[0]); }
                        if (n == 3) { x[1] = opqf(p[1] + q[1]); x[2] = opqf(p[2] + q[2]); x[3] = opqf(p[3] + q[3]); }
                        if (n == 4) { xp[pr - 1][0] = pkc(x[0], x[1]); xp[pr - 1][1] = pkc(x[2], x[3]); }
                        if (n == 5 && !(pr & 1)) gst(c, pr == 2 ? zo : zo1, za, z_pack(xp[pr - 2], xp[pr - 1]));
                        if (n >= 4 && pr < 3) { f4& z = acc[2 * pr + n - 2]; z = f4{0, 0, 0, 0}; asm volatile("" : "+v"(z)); }
                    }
                    SB();
                }
            }
            __builtin_amdgcn_s_setprio(0);
            TS(c, 3);
            if (c.h < 20 && s < NSEG - 1) { node_u<UPRE, 8>(c, na, u); node_duty<NSLEEP>(c, na, u); }
        } else {
            if (s >= -2) get_mask(c);
            TS(c, 6);
            barv();
            TS(c, 5);
        }
        const bool bulk = s >= -2 && s + 2 < NSEG;
        if (bulk) {
            __builtin_amdgcn_s_setprio(UPRIO);
            bulk_gemm2(c, W, s + 2);
            __builtin_amdgcn_s_setprio(0);
            const int l = lane_id(), g = l >> 4;
            const v4u sv = sentv();
            const u32 za = z_lane(l);
#pragma unroll
            for (int k = 0; k < 3; ++k)
                if (k < 2 || !(g & 1)) gst(c, zb(c, s + 2) + k * 262144 + (c.wave & 3) * 16384 + c.h * 256, za, sv);
        }
        auto flag_u = [&] { if (bulk) { asm volatile("s_waitcnt vmcnt(0)" ::: "memory"); gst32(c, fb(c, s + 2) + (c.wave & 3) * 128 + c.h * 4, 0, opq(1u)); } };
        if (out_vm(c) && s >= 2) {
            f4 a[2];
            TS(c, 5);
            out_part(c, s - 2, c.h + 12, a, [] {});
            flag_u();
            TS(c, 0);
            bar();
            TS(c, 1);
            const u32 l16 = lane_id() * 16, pa = V_MEM + (c.wave - 4) * 2048 + l16;
            a[0] += __builtin_bit_cast(f4, lds128(pa)); a[1] += __builtin_bit_cast(f4, lds128(pa + 1024));
            const u32 pb = V_X + 4096 + (c.wave >= 6 ? c.wave - 5 : 0) * 8192 + l16;
            if (c.wave != 4) st128(pb, __builtin_bit_cast(v4u, a[0]));
            if (c.wave != 5) st128(pb + 1024, __builtin_bit_cast(v4u, a[1]));
            bar();
            if (c.wave < 6) {
                const u32 pc = V_X + 4096 + (c.wave & 1) * 1024 + l16;
                f4 r = (c.wave & 1) ? a[1] : a[0];
                r += __builtin_bit_cast(f4, lds128(pc));
                r += __builtin_bit_cast(f4, lds128(pc + 8192)) + __builtin_bit_cast(f4, lds128(pc + 16384));
                out_store(c, s - 2, c.h + 12, c.wave & 1, r);
            }
        } else {
            TS(c, 0);
            bar();
            TS(c, 1);
            flag_u();
        }
        TS(c, 4);
        vm_duty(c, s);
        TSF(c, s);
    }
}

__global__ void __launch_bounds__(512, 2) memtr_kernel(Args a) {
    Ctx c;
    const int wg = blockIdx.x;
    c.wave = __builtin_amdgcn_readfirstlane(threadIdx.x >> 6);
    const int br = (wg & 7) >> 2;
    const int idx = (wg >> 3) * 4 + (wg & 3);
    c.h = idx & 31; c.p = idx >> 6;
    c.r = __builtin_amdgcn_make_buffer_rsrc(a.ws, 0, WS_SPAN, 0x00020000);
    c.par = br ? ~0u : 0u;
    c.spin = 0;
    c.x = br ? a.x[1] : a.x[0]; c.mem0 = br ? a.mem0[1] : a.mem0[0]; c.out = a.out + br * 640;
    if (((idx >> 5) & 1) == 0) qk_main(c, br ? a.w[1][0] : a.w[0][0], br ? a.w[1][1] : a.w[0][1]);
    else {
        if (c.h >= 16) prefetch_x(c, c.p + ((c.h >> 2) & 3) * 2, V_XL + 7 * 2048);
        else if (c.h >= 12 && c.wave < 5) {
            const unsigned char* m = (const unsigned char*)c.mem0 + c.wave * 2048;
            dmai<0, 0>(m, lane_id() * 16, V_XL + 7 * 2048); dmai<1024, 0>(m, lane_id() * 16, V_XL + 7 * 2048);
        }
        if (c.wave < 7) { load_x<1, 1>(c, c.p, 8 + c.wave, 0, V_XL); load_x<1, 1>(c, c.p, 8 + c.wave, 1, V_XL); }
        if (c.wave < 4) v_main(c, br ? a.w[1][2] : a.w[0][2]);
        else o_main(c, br ? a.w[1][3] : a.w[0][3]);
    }
}

extern "C" void kernel_launch(void* const* d_in, const int* in_sizes, int n_in, void* d_out, int out_size, void* d_ws, size_t ws_size, hipStream_t stream) {
    Args a;
    for (int b = 0; b < 2; ++b) {
        a.x[b] = (const u16*)d_in[7 * b]; a.mem0[b] = (const u16*)d_in[7 * b + 1];
        for (int k = 0; k < 4; ++k) a.w[b][k] = (const u16*)d_in[7 * b + 2 + k];
    }
    a.out = (u16*)d_out; a.ws = (unsigned char*)d_ws;
    (void)hipMemsetAsync((unsigned char*)d_ws + INIT_OFF, 0xFF, INIT_BYTES, stream);
    memtr_kernel<<<256, 512, 0, stream>>>(a);
}
```

```cpp
#include <hip/hip_runtime.h>
#include <stdint.h>

typedef unsigned u32;
typedef unsigned short u16;
typedef u32 v2u __attribute__((ext_vector_type(2)));
typedef u32 v4u __attribute__((ext_vector_type(4)));
typedef u32 v8u __attribute__((ext_vector_type(8)));
typedef float f4 __attribute__((ext_vector_type(4)));
typedef float f2 __attribute__((ext_vector_type(2)));
typedef short s4 __attribute__((ext_vector_type(4)));
typedef __bf16 b2 __attribute__((ext_vector_type(2)));
typedef __bf16 b8 __attribute__((ext_vector_type(8)));
typedef __bf16 b16 __attribute__((ext_vector_type(16)));
#define AS3 __attribute__((address_space(3)))
#define DI __device__ __forceinline__

constexpr int NSEG = 64;
constexpr u32 SENT = 0xFFFFFFFFu;
constexpr int NPAGE = 17;
constexpr u32 WS_SPAN = NPAGE * 2097152;
constexpr u32 INIT_OFF = 2097152 - 147456, INIT_BYTES = 147456 + 5 * 65536;
constexpr u32 MP_PM = 20480, MP_PX = 32768;
constexpr u32 L_IMG = 98304, L_X = 122880, L_MEM = L_X + 24576, L_F = L_X + 34816;
constexpr u32 V_MEM = L_IMG + 12288, V_VS = V_MEM + 10240, V_DUMP = V_VS + 3072, V_X = V_DUMP + 1024;
#define QSLEEP 5
#define UPRIO 1
#define QM0 7
#define QM1 1
#define NSLEEP 5
#define OSLEEP 0
#define OVSLEEP 0
#define UPRE 6
#define VPRE 4
#define VPRIO 3
#define OPRIO 3
#define PSLEEP 3
#ifndef NVX
#define NVX 4
#endif
#define VSLEEP 0
constexpr u32 SPIN_MAX = 1u << 20;

struct Args { const u16* x[2]; const u16* mem0[2]; const u16* w[2][4]; u16* out; unsigned char* ws; };

__shared__ __attribute__((aligned(1024))) unsigned char lds[163840];

DI v4u lds128(u32 a) { return *(AS3 v4u*)((AS3 unsigned char*)lds + a); }
DI f2 lds64f(u32 a) { return *(AS3 f2*)((AS3 unsigned char*)lds + a); }
DI void st128(u32 a, v4u v) { *(AS3 v4u*)((AS3 unsigned char*)lds + a) = v; }
DI void st64(u32 a, v2u v) { *(AS3 v2u*)((AS3 unsigned char*)lds + a) = v; }
DI void st32(u32 a, u32 v) { *(AS3 u32*)((AS3 unsigned char*)lds + a) = v; }
DI v2u ldtr(u32 a) { return __builtin_bit_cast(v2u, __builtin_amdgcn_ds_read_tr16_b64_v4i16((AS3 s4*)((AS3 unsigned char*)lds + a))); }
struct TB { u32 a, b; };
DI u32 opq(u32 v);
DI TB mktb(u32 a) { TB t = {opq(a), opq(a ^ 128u)}; return t; }
DI v4u trfrag(TB t, u32 off) { v2u lo = ldtr(t.a + off), hi = ldtr(t.b + off); v4u r = {lo.x, lo.y, hi.x, hi.y}; return r; }
DI int lane_id() { int l; asm volatile("v_mbcnt_lo_u32_b32 %0, -1, 0\n\tv_mbcnt_hi_u32_b32 %0, -1, %0" : "=v"(l)); return l; }
DI u32 opq(u32 v) { asm volatile("" : "+v"(v)); return v; }
DI float opqf(float v) { asm volatile("" : "+v"(v)); return v; }
DI v4u sentv() { const u32 v = opq(SENT); v4u r = {v, v, v, v}; return r; }
DI u32 rowp(int l) { return ((((l & 15) >> 2) ^ ((l >> 2) & 4)) * 32 + (l & 3) * 8); }
DI v8u cat8(v4u a, v4u b) { return __builtin_shufflevector(a, b, 0, 1, 2, 3, 4, 5, 6, 7); }
DI v4u lo4(v8u a) { return __builtin_shufflevector(a, a, 0, 1, 2, 3); }
DI v4u hi4(v8u a) { return __builtin_shufflevector(a, a, 4, 5, 6, 7); }
DI f4 mfma(v4u a, v4u b, f4 c) { return __builtin_amdgcn_mfma_f32_16x16x32_bf16(__builtin_bit_cast(b8, a), __builtin_bit_cast(b8, b), c, 0, 0, 0); }
DI f4 smf(v4u a, v8u b, f4 c, u32 idx) { return __builtin_amdgcn_smfmac_f32_16x16x64_bf16(__builtin_bit_cast(b8, a), __builtin_bit_cast(b16, b), c, (int)idx, 0, 0); }
DI u32 pk(float a, float b) { b2 r = {(__bf16)a, (__bf16)b}; return __builtin_bit_cast(u32, r); }
DI u32 pkc(float a, float b) { u32 r = pk(a, b); return r < 0xFFFFFFFEu ? r : 0xFFFFFFFEu; }
DI void bar() { asm volatile("s_waitcnt lgkmcnt(0)\n\ts_barrier" ::: "memory"); }
DI void barv() { asm volatile("s_waitcnt vmcnt(0) lgkmcnt(0)\n\ts_barrier" ::: "memory"); }
template <bool LOW, bool VMEM> DI void bar_half(int wave) {
    if (VMEM) asm volatile("s_cmp_lt_u32 %0, 4\n\ts_cbranch_scc%1 LBH%=\n\ts_waitcnt vmcnt(0) lgkmcnt(0)\n\ts_barrier\nLBH%=:" :: "s"(wave), "n"(LOW ? 0 : 1) : "memory", "scc");
    else asm volatile("s_cmp_lt_u32 %0, 4\n\ts_cbranch_scc%1 LBH%=\n\ts_waitcnt lgkmcnt(0)\n\ts_barrier\nLBH%=:" :: "s"(wave), "n"(LOW ? 0 : 1) : "memory", "scc");
}
#define WHINT " nt"
#define PROUNDS 3
#define QB 7
#define MPF 3
#define MPRE 1
#define BSLEEP 0
#define OAUX 2
DI void dma(const void* sbase, u32 voff, u32 ldst) {
    asm volatile("s_mov_b32 m0, %2\n\ts_nop 0\n\tglobal_load_lds_dwordx4 %0, %1" WHINT :: "v"(voff), "s"(sbase), "s"(ldst) : "memory");
}
template <int GO, int LO> DI void dmai(const void* sbase, u32 voff, u32 ldst) {
    asm volatile("s_add_u32 m0, %2, %3\n\ts_nop 0\n\tglobal_load_lds_dwordx4 %0, %1 offset:%4" :: "v"(voff), "s"(sbase), "s"(ldst), "n"(LO - GO), "n"(GO) : "memory", "scc");
}
DI float fold32(float a, float b) { auto s = __builtin_amdgcn_permlane32_swap(__float_as_uint(a), __float_as_uint(b), false, false); return __uint_as_float(s[0]) + __uint_as_float(s[1]); }
DI float fmx(float a, float b) { return __builtin_elementwise_maximum(a, b); }
DI float xmax(float x) {
    auto s = __builtin_amdgcn_permlane16_swap(__float_as_uint(x), __float_as_uint(x), false, false); x = fmx(__uint_as_float(s[0]), __uint_as_float(s[1]));
    auto t = __builtin_amdgcn_permlane32_swap(__float_as_uint(x), __float_as_uint(x), false, false); return fmx(__uint_as_float(t[0]), __uint_as_float(t[1]));
}
DI float xsum(float x) {
    auto s = __builtin_amdgcn_permlane16_swap(__float_as_uint(x), __float_as_uint(x), false, false); x = __uint_as_float(s[0]) + __uint_as_float(s[1]);
    auto t = __builtin_amdgcn_permlane32_swap(__float_as_uint(x), __float_as_uint(x), false, false); return __uint_as_float(t[0]) + __uint_as_float(t[1]);
}
DI float ror8(float x) { return __uint_as_float(__builtin_amdgcn_update_dpp(0, __float_as_uint(x), 0x128, 0xF, 0xF, true)); }
DI u32 umax(u32 a, u32 b) { return a > b ? a : b; }
DI u32 max4(v4u v) { return umax(umax(v.x, v.y), umax(v.z, v.w)); }

struct Ctx {
    __amdgpu_buffer_rsrc_t r; u32 par; int h, p, wave; u32 spin;
    const u16* x; const u16* mem0; u16* out;
};
DI constexpr u32 io(u32 o) { return ((o >> 13) << 14) | (o & 8191u); }
DI u32 pgb(const Ctx& c, u32 page) { return (page << 21) | (((c.par >> page) & 1u) << 13); }
DI u32 mpb(const Ctx& c, int s) { return pgb(c, 1 + ((u32)s >> 5)) + ((u32)s & 31u) * 65536u; }
DI u32 zb(const Ctx& c, int s) { const u32 b = (u32)s & 7u, q = (b * 11u) >> 5; return pgb(c, 3 + q) + (b - 3u * q) * 655360u; }
DI u32 ub(const Ctx& c, int s, u32 w) { const u32 b = ((u32)s & 7u) * 4u + w, q = (b * 43u) >> 7; return pgb(c, 6 + q) + (b - 3u * q) * 655360u; }
DI u32 pxb(const Ctx& c, int s) { return pgb(c, 0) + ((u32)s % 6u) * 163840u; }
DI u32 fb(const Ctx& c, int s) { return pgb(c, 0) + (INIT_OFF + 16384) + io((u32)s * 1024u); }
DI v4u gld(const Ctx& c, u32 off, u32 voff) { return __builtin_amdgcn_raw_buffer_load_b128(c.r, voff, off, 16); }
DI void gst(const Ctx& c, u32 off, u32 voff, v4u v) { __builtin_amdgcn_raw_buffer_store_b128(v, c.r, voff, off, 16); }
DI void gst64(const Ctx& c, u32 off, u32 voff, v2u v) { __builtin_amdgcn_raw_buffer_store_b64(v, c.r, voff, off, 16); }
DI void gst32(const Ctx& c, u32 off, u32 voff, u32 v) { __builtin_amdgcn_raw_buffer_store_b32(v, c.r, voff, off, 16); }

#define TS(c, k)
#define TSF(c, s)
#define TS1(c, k) TS(c, k)
#define TS2(c, k)
#define SB() __builtin_amdgcn_sched_barrier(0)

template <int N> DI void poll(Ctx& c, const u32 (&off)[N], const u32 (&voff)[N], v4u (&v)[N]) {
    for (;;) {
        asm volatile("" ::: "memory");
        u32 m = 0;
#pragma unroll
        for (int k = 0; k < N; ++k) v[k] = gld(c, off[k], voff[k]);
#pragma unroll
        for (int k = 0; k < N; ++k) m = umax(m, max4(v[k]));
        if (__builtin_amdgcn_ballot_w64(m == SENT) == 0) return;
        if (++c.spin > SPIN_MAX) return;
    }
}

DI void probe_prep(const Ctx& c, int pg) {
    const int l = lane_id();
    __builtin_amdgcn_raw_buffer_store_b32(opq(SENT), c.r, (l & 1) * 8192 + ((l >> 1) & 3) * 256, pg * 2097152 + 1048576, 16);
}
DI void probe_page(const Ctx& c, int pg) {
    u32 best[2] = {~0u, ~0u};
    const u32 o = pg * 2097152 + 1048576;
    for (int r = 0; r < PROUNDS; ++r) {
#pragma unroll
        for (int k = 0; k < 2; ++k) {
            const u32 t0 = (u32)__builtin_amdgcn_s_memtime();
            const u32 v = __builtin_amdgcn_raw_buffer_load_b32(c.r, 0, o + k * 8192 + r * 256, 16);
            asm volatile("s_waitcnt vmcnt(0)" :: "v"(v) : "memory");
            const u32 d = (u32)__builtin_amdgcn_s_memtime() - t0;
            best[k] = d < best[k] ? d : best[k];
        }
    }
    __builtin_amdgcn_raw_buffer_store_b32(opq(best[1] < best[0] ? 1u : 0u), c.r, 0, INIT_OFF + pg * 4, 16);
}
DI void get_mask(Ctx& c) {
    const int l = lane_id();
    const u32 vo = (l < NPAGE ? l : 0) * 4;
    for (;;) {
        asm volatile("" ::: "memory");
        const u32 v = __builtin_amdgcn_raw_buffer_load_b32(c.r, vo, INIT_OFF, 16);
        if (__builtin_amdgcn_ballot_w64(v == SENT) == 0 || ++c.spin > SPIN_MAX) { c.par ^= (u32)__builtin_amdgcn_ballot_w64(v == 1u); return; }
    }
}

template <int N, int ST, int I = 0> DI void dma_x(const unsigned char* b, u32 voff, u32 dst) {
    if constexpr (I < N) { dmai<I * ST * 64, I * ST * 2048>(b, voff, dst); dma_x<N, ST, I + 1>(b, voff, dst); }
}
template <int N, int ST> DI void load_x(const Ctx& c, int s, int ks, int m, u32 dst) {
    const int l = lane_id();
    dma_x<N, ST>((const unsigned char*)c.x + (s * 40960 + m * 20480 + ks * 64), (l >> 2) * 1280 + (((l & 3) ^ (-(l >> 4) & 3)) << 4), dst + ks * 2048 + m * 1024);
}
DI u32 xfrag(int l) { return (l & 15) * 64 + (((l >> 4) ^ (-(l >> 2) & 3)) << 4); }
constexpr u32 V_XH = V_X - 16 * 2048, V_XL = V_X + 4 * 2048, L_XH = L_X - 16 * 2048, L_XL = L_X + 4 * 2048;
DI void prefetch_x(const Ctx& c, int s, u32 dump, int part = 0) {
    const unsigned char* b = (const unsigned char*)c.x + (s * 40960 + (c.wave + part) * 5120);
    const u32 voff = lane_id() * 16;
    dmai<0, 0>(b, voff, dump); dmai<1024, 0>(b, voff, dump); dmai<2048, 0>(b, voff, dump); dmai<3072, 0>(b, voff, dump); dmai<0, 0>(b + 4096, voff, dump);
}
DI u32 flag_load(const Ctx& c, int s, bool vm) {
    const int l = lane_id();
    const bool node = vm && c.h < 20;
    const u32 w = node ? (c.h & 1) * 2 : (c.h & 3), d = node ? 128 : 512 - w * 128;
    return __builtin_amdgcn_raw_buffer_load_b32(c.r, (l & 3) * 4 + ((l & 4) ? d : 0) + (node ? (l & 8) * 8 : 0), fb(c, s < 0 ? 0 : s) + w * 128 + (node ? c.wave & 3 : c.wave) * 16, 16);
}
DI u32 pending(u32 v) { u32 r = (u32)__builtin_amdgcn_ballot_w64(v == SENT); asm volatile("" : "+s"(r)); return r; }
DI void flag_wait(Ctx& c, int s, bool vm, u32 miss) {
    while (miss != 0 && ++c.spin < SPIN_MAX) { asm volatile("" ::: "memory"); miss = pending(flag_load(c, s, vm)); }
    asm volatile("" ::: "memory");
}

DI u32 mem_in(const Ctx& c, int s) { return mpb(c, s) + io((c.wave < 4 ? c.wave : 4) * 2048); }
template <u32 LM> DI void stage_mem(Ctx& c, int s, u32 o) {
    if (c.wave < 4 || c.wave == (LM == L_MEM ? 4 : 7)) {
        const int l = lane_id(), sl = c.wave < 4 ? c.wave : 4;
        v4u v[2];
        if (s == 0) {
            const __amdgpu_buffer_rsrc_t mr = __builtin_amdgcn_make_buffer_rsrc((void*)c.mem0, 0, 10240, 0x00020000);
            const u32 m = (l & 7) * 1280 + (l >> 4) * 32 + (l & 8) / 2;
#pragma unroll
            for (int k = 0; k < 2; ++k)
#pragma unroll
                for (int j = 0; j < 4; ++j) v[k][j] = __builtin_amdgcn_raw_buffer_load_b32(mr, m + k * 128 + j * 8, sl * 256, 0);
        } else {
            const u32 off[2] = {o, o}; const u32 voff[2] = {(u32)l * 16, (u32)l * 16 + 1024};
            poll<2>(c, off, voff, v);
        }
        st128(LM + sl * 2048 + l * 16, v[0]);
        st128(LM + sl * 2048 + 1024 + l * 16, v[1]);
    }
    if (LM == L_MEM) barv(); else bar();
}

struct NodeA { u32 u0, u1, px, pm, z, mo, mi, zo; };
DI NodeA node_addr(const Ctx& c, int t) {
    const u32 n = c.h, uo = (n >> 1) * 65536 + io(c.wave * 4096), po = mpb(c, t);
    NodeA a = {ub(c, t, (2 * n) & 3) + uo, ub(c, t, (2 * n + 1) & 3) + uo, po + MP_PX + io(c.wave * 2048), po + MP_PM + c.wave * 512,
               zb(c, t) + n * 32768 + c.wave * 1024, mpb(c, t + 1) + io((2 * n + (c.wave & 1)) * 256),
               po + io((c.wave < 4 ? c.wave : 4) * 2048), zb(c, t) + (c.wave & 3) * 16384 + n * 256};
    asm volatile("" : "+s"(a.u0), "+s"(a.u1), "+s"(a.px), "+s"(a.pm), "+s"(a.z), "+s"(a.mo), "+s"(a.mi), "+s"(a.zo));
    return a;
}
template <int I0, int I1> DI void node_u(const Ctx& c, const NodeA& na, v4u (&u)[2][4]) {
    const u32 l16 = lane_id() * 16 + (c.h < 20 ? 0u : 0x80000000u);
#pragma unroll
    for (int i = I0; i < I1; ++i) u[i >> 2][i & 3] = gld(c, i >> 2 ? na.u1 : na.u0, l16 + (i & 3) * 1024);
}
template <int SLP> DI void node_duty(Ctx& c, const NodeA& na, v4u (&u)[2][4]) {
    const int l = lane_id();
    const u32 l16 = l * 16;
    const u32 mo = na.mo;
    const u32 off[3] = {na.px, na.px, na.pm}, zoff[2] = {na.z, na.z + 16384};
    const u32 voff[3] = {l16, l16 + 1024, (u32)((l >> 4) * 128 + (l & 7) * 16)}, zvoff[2] = {l16, l16};
    v4u v[3], zz[2];
    const int jj = l & 3, r16 = (l >> 2) ^ ((jj >> 1) << 3);
    const u32 a = opq(V_MEM + (jj * 16 + ((c.wave ^ jj) & 1) * 8 + (r16 & 7)) * 16 + (r16 >> 3) * 8);
    const u32 ra = opq(V_MEM + c.wave * 1024 + (l16 ^ ((l & 16) << 3))), l4 = opq(r16 * 16 + jj * 4);
    __builtin_amdgcn_s_setprio(3);
    __builtin_amdgcn_s_sleep(SLP);
    TS(c, 5);
    zz[0] = gld(c, zoff[0], l16); zz[1] = gld(c, zoff[1], l16);
    __builtin_amdgcn_s_sleep(PSLEEP);
    poll<3>(c, off, voff, v);
    if (__builtin_amdgcn_ballot_w64(umax(max4(zz[0]), max4(zz[1])) == SENT) != 0) { ++c.spin; poll<2>(c, zoff, zvoff, zz); }
    TS(c, 6);
    const f4 z = {0, 0, 0, 0};
    f4 lo1 = mfma(u[1][0], v[0], z), hi0 = mfma(u[0][1], v[0], z);
    lo1 = mfma(u[1][2], v[1], lo1); hi0 = mfma(u[0][3], v[1], hi0);
    lo1 = mfma(zz[1], v[2], lo1);
    f4 lo0 = mfma(u[0][0], v[0], z), hi1 = mfma(u[1][1], v[0], z);
    SB();
    f4 r, w;
    lo0 = mfma(u[0][2], v[1], lo0);
#pragma unroll
    for (int i = 0; i < 4; ++i) w[i] = opqf((l & 8) ? hi0[i] : lo1[i]);
    SB();
    hi1 = mfma(u[1][3], v[1], hi1);
    lo0 = mfma(zz[0], v[2], lo0);
    SB();
#pragma unroll
    for (int i = 0; i < 4; ++i) r[i] = opqf(((l & 8) ? hi1[i] : lo0[i]) + ror8(w[i]));
    st128(ra, __builtin_bit_cast(v4u, r));
    bar();
    if (c.wave < 2) {
        f2 q[8];
#pragma unroll
        for (int w = 0; w < 8; ++w) q[w] = lds64f(a + w * 1024);
        const f2 s = ((q[0] + q[1]) + (q[2] + q[3])) + ((q[4] + q[5]) + (q[6] + q[7]));
        gst32(c, mo, l4, pkc(s.x, s.y));
    } else __builtin_amdgcn_s_sleep(BSLEEP);
    __builtin_amdgcn_s_setprio(0);
    TS(c, 7);
}

template <class F> DI void out_part(Ctx& c, int t, int j, f4 (&a)[2], F mid) {
    const u32 l16 = lane_id() * 16;
    a[0] = f4{0, 0, 0, 0}; a[1] = f4{0, 0, 0, 0};
    v4u u[4], px[4][2], z[3];
#pragma unroll
    for (int k = 0; k < 4; ++k) {
        u[k] = gld(c, ub(c, t, j & 3) + (j >> 2) * 65536 + io(c.wave * 4096), l16 + k * 1024);
        px[k][0] = gld(c, pxb(c, t) + c.wave * 16384 + (k >> 1) * 4096, l16 + (k & 1) * 2048);
        px[k][1] = gld(c, pxb(c, t) + c.wave * 16384 + (k >> 1) * 4096, l16 + (k & 1) * 2048 + 1024);
    }
    const u32 po = pxb(c, t) + 131072 + io(c.wave * 2048);
    const u32 off[1] = {zb(c, t) + j * 16384 + c.wave * 1024}; const u32 voff[1] = {l16};
    SB();
    mid();
    SB();
    a[0] = mfma(u[0], px[0][0], a[0]); a[1] = mfma(u[0], px[0][1], a[1]);
    SB();
    z[0] = gld(c, off[0], l16); z[1] = gld(c, po, l16); z[2] = gld(c, po, l16 + 1024);
    SB();
#pragma unroll
    for (int k = 1; k < 4; ++k) { a[0] = mfma(u[k], px[k][0], a[0]); a[1] = mfma(u[k], px[k][1], a[1]); }
    SB();
    if (__builtin_amdgcn_ballot_w64(max4(z[0]) == SENT) != 0) { v4u zz[1]; poll<1>(c, off, voff, zz); z[0] = zz[0]; }
    a[0] = mfma(z[0], z[1], a[0]); a[1] = mfma(z[0], z[2], a[1]);
}
DI void out_store(const Ctx& c, int t, int j, int m, f4 s) {
    const int l = lane_id();
    v2u o = {pk(s[0], s[1]), pk(s[2], s[3])};
    const __amdgpu_buffer_rsrc_t orr = __builtin_amdgcn_make_buffer_rsrc((void*)c.out, 0, 2048 * 2560 - 1280, 0x00020000);
    __builtin_amdgcn_raw_buffer_store_b64(o, orr, (l & 15) * 2560 + (l >> 4) * 8, (t * 32 + m * 16) * 2560 + j * 32, OAUX);
}
template <u32 LR, class F> DI void out_duty(Ctx& c, int t, int j, F mid) {
    f4 a[2];
    out_part(c, t, j, a, [] {});
    const u32 l16 = lane_id() * 16;
    mid();
    if (c.wave != 0) st128(LR + (c.wave - 1) * 1024 + l16, __builtin_bit_cast(v4u, a[0]));
    if (c.wave != 1) st128(LR + 7168 + (c.wave ? c.wave - 1 : 0) * 1024 + l16, __builtin_bit_cast(v4u, a[1]));
    bar();
    if (c.wave < 2) {
        f4 s = c.wave ? a[1] : a[0];
#pragma unroll
        for (int w = 0; w < 7; ++w) s += __builtin_bit_cast(f4, lds128(LR + c.wave * 7168 + w * 1024 + l16));
        out_store(c, t, j, c.wave, s);
    }
    bar();
}

template <bool WO> DI TB wbase(const Ctx& c, int l) {
    return mktb(WO ? 12288 + (l >> 4) * 1024 + (c.wave & 3) * 256 + rowp(l) : (c.wave >> 2) * 12288 + (l >> 4) * 3072 + (c.wave & 3) * 768 + rowp(l));
}
template <bool WO> DI void issue_chunk(const unsigned char* wb, u32 voff, int wave, int ch) {
    const u32 slot = ((ch + 2) % 6) * 24576;
#pragma unroll
    for (int u = 0; u < 3; ++u) {
        if (WO) dma(wb + (size_t)(3 * (ch & 1) + u) * 40960 + (ch >> 1) * 128, voff, slot + 12288 + (u * 4 + (wave & 3)) * 1024);
        else dma(wb + (size_t)ch * 393216 + u * 128, voff, slot + (wave >> 2) * 12288 + (wave & 3) * 3072 + u * 1024);
    }
}
template <bool WO> DI void load_weights(const Ctx& c, const u16* w, v8u (&W)[24]) {
    const int l = lane_id();
    const int kb = c.wave & 3, rr = ((l >> 1) & 7) ^ (4 * (kb & 1));
    const u32 voff = (8 * kb + rr) * (WO ? 1280 : 12288) + (l >> 4) * 32 + (l & 1) * 16;
    const unsigned char* wb = (const unsigned char*)w + (WO ? (size_t)c.h * 245760 : (size_t)c.h * 384);
    const TB wl = wbase<WO>(c, l);
#pragma unroll
    for (int i = 0; i < 5; ++i) issue_chunk<WO>(wb, voff, c.wave, i);
#pragma unroll
    for (int i = 0; i < 16; ++i) {
        asm volatile("s_waitcnt vmcnt(12) lgkmcnt(0)\n\ts_barrier" ::: "memory");
        if (i < 15) issue_chunk<WO>(wb, voff, c.wave, i + 5);
        const u32 slot = ((i + 2) % 6) * 24576;
#pragma unroll
        for (int u = 0; u < 3; ++u) {
            v4u f = trfrag(wl, slot + u * (WO ? 4096 : 256));
            const int ks = 3 * (i & 1) + u;
            const int pi = WO ? (i >> 1) * 3 + (ks >> 1) : u * 8 + (i >> 1), hf = WO ? (ks & 1) : (i & 1);
            W[pi] = hf ? cat8(lo4(W[pi]), f) : cat8(f, f);
        }
    }
    barv();
}

template <u32 LM, class F> DI u32 crit_gemm1(Ctx& c, int s, const v8u (&W)[24], int l, u32 d01, u32 d2, u32 mi, F late) {
    TB wl = wbase<false>(c, l);
    v8u T[3];
#pragma unroll
    for (int u = 0; u < 3; ++u) T[u] = cat8(trfrag(wl, u * 256), trfrag(wl, 24576 + u * 256));
    wl.a = opq(wl.a + 49152); wl.b = opq(wl.b + 49152);
    const u32 idx = opq(0x4444u + (l & 8) * 0x1555u + ((l & 8) >> 2) + (LM == L_MEM ? ((l >> 4) * 256 + rowp(l)) << 16 : 0u)), ab = opq(LM + l * 16);
    d01 = opq(d01); d2 = opq(d2);
    f4 acc[3] = {{0, 0, 0, 0}, {0, 0, 0, 0}, {0, 0, 0, 0}};
    asm volatile("" : "+v"(acc[0]), "+v"(acc[1]), "+v"(acc[2]));
    TS(c, 0);
    stage_mem<LM>(c, s, mi);
    TS(c, 1);
    v4u a[11];
    a[0] = lds128(ab + 8192);
    SB();
#pragma unroll
    for (int i = 1; i < 4; ++i) a[i] = lds128(ab + (i - 1) * 1024);
    SB();
#pragma unroll
    for (int i = 0; i < 10; ++i) {
#pragma unroll
        for (int u = 0; u < 3; ++u) acc[u] = smf(a[i], (i == 0 || i == 9) ? T[u] : W[u * 8 + i - 1], acc[u], idx);
        SB();
        if (i + 4 < 10) a[i + 4] = lds128(ab + (i + 4 == 9 ? 9 : i + 3) * 1024);
        if (i >= 3 && i < 6) T[i - 3] = cat8(trfrag(wl, (i - 3) * 256), trfrag(wl, 24576 + (i - 3) * 256));
        late(i);
        SB();
        if (LM == L_MEM && i == 0) TS2(c, 5);
    }
    if (LM == L_MEM) TS2(c, 6);
    float r[4];
#pragma unroll
    for (int i = 0; i < 4; ++i) r[i] = fold32(acc[0][i], acc[1][i]);
    st64(d01, v2u{pk(r[0], r[1]), pk(r[2], r[3])});
    st32(d2, pk(fold32(acc[2][0], acc[2][2]), fold32(acc[2][1], acc[2][3])));
    if (LM == L_MEM) TS2(c, 7);
    SB();
    return idx >> 16;
}

DI constexpr int step_ks(int q) { return q < 8 ? ((q & 1) ? 16 + (q >> 1) : (q >> 1)) : q - 4; }
DI constexpr u32 step_slot(int q) { return (step_ks(q) >= 16 ? step_ks(q) - 16 : step_ks(q) + 4) * 2048; }
template <bool VM, class F> DI void bulk_gemm1(Ctx& c, const v8u (&W)[24], int s, F hook) {
    hook(-1);
    const int l = lane_id();
    const TB wl = wbase<false>(c, l);
    const u32 ab = opq((VM ? V_X : L_X) + xfrag(l));
    f4 acc[2][3];
#pragma unroll
    for (int m = 0; m < 2; ++m)
#pragma unroll
        for (int u = 0; u < 3; ++u) acc[m][u] = f4{0, 0, 0, 0};
    v4u a[22][2], b[5][3];
#pragma unroll
    for (int i = 0; i < 4; ++i) a[i >> 1][i & 1] = lds128(ab + step_slot(i >> 1) + (i & 1) * 1024);
#pragma unroll
    for (int u = 0; u < 3; ++u) b[0][u] = trfrag(wl, u * 256);
#pragma unroll
    for (int q = 0; q < 20; ++q) {
        const int ks = step_ks(q);
        if (VM && q == 10) {
            const int l2 = lane_id();
            const unsigned char* g = (const unsigned char*)c.x + (size_t)s * 40960 + 960;
            const u32 vo = (l2 & 15) * 1280 + (l2 >> 4) * 16;
            a[19][0] = *(const v4u*)(g + vo); a[19][1] = *(const v4u*)(g + 20480 + vo);
        }
        hook(q);
        SB();
#pragma unroll
        for (int m = 0; m < 2; ++m) {
#pragma unroll
            for (int u = 0; u < 3; ++u) {
                acc[m][u] = mfma(a[q][m], ks >= 16 ? b[ks - 16][u] : ((ks & 1) ? hi4(W[u * 8 + (ks >> 1)]) : lo4(W[u * 8 + (ks >> 1)])), acc[m][u]);
                if (m == 1 && ks >= 16 && ks < 19) { SB(); b[ks - 15][u] = trfrag(wl, (ks - 15) * 24576 + u * 256); SB(); }
            }
            SB();
            if (q < (VM ? 17 : 18)) a[q + 2][m] = lds128(ab + step_slot(q + 2) + m * 1024);
            SB();
        }
    }
    const int l3 = lane_id(), cc = l3 & 15;
    const u32 ia = L_IMG + (c.wave >> 2) * 12288 + ((c.wave & 3) * 48 + (cc ^ (4 * (cc >> 3)))) * 32 + (l3 >> 4) * 8;
    v2u v[2][3];
#pragma unroll
    for (int m = 0; m < 2; ++m)
#pragma unroll
        for (int u = 0; u < 3; ++u) v[m][u] = v2u{pk(acc[m][u][0], acc[m][u][1]), pk(acc[m][u][2], acc[m][u][3])};
    hook(20);
#pragma unroll
    for (int m = 0; m < 2; ++m)
#pragma unroll
        for (int u = 0; u < 3; ++u) st64(ia + m * 6144 + u * 512, v[m][u]);
}

DI bool out_vm(const Ctx& c) { return c.h >= 20 && c.h < 28; }
DI void vm_duty(Ctx& c, int s) {
    if (s + 6 < NSEG && c.wave >= 4 && c.wave < 7) {
        load_x<2, 2>(c, s + 6, 16 + ((c.wave >> 1) & 1), c.wave & 1, V_XH);
        load_x<7, 2>(c, s + 6, (c.wave >> 1) & 1, c.wave & 1, V_XL);
        if (c.wave < 6) { load_x<1, 1>(c, s + 6, 14, c.wave & 1, V_XL); load_x<3, 2>(c, s + 6, 1 + (c.wave & 1) * 6, 1, V_XL); }
        else { load_x<2, 2>(c, s + 6, 17, 1, V_XH); load_x<1, 1>(c, s + 6, 13, 1, V_XL); }
    }
    if (out_vm(c) && s >= 0) {
        flag_wait(c, s, true, 1);
        if (s + 2 >= NSEG) out_duty<V_MEM>(c, s, c.h + 12, [] {});
    }
}

template <bool MEM> DI void attn8(Ctx& c, int s, u32 pa) {
    v4u k[6][2], q[6], m[6];
    constexpr int PF = MEM ? MPF : 3;
    if (!MEM) bar();
    const TB il = mktb(L_IMG + 12288 + pa), fl = mktb(L_F + (pa & ~16u));
#pragma unroll
    for (int ks = 0; ks < (MEM ? MPRE : PF); ++ks) { k[ks][0] = trfrag(il, ks * 1024); k[ks][1] = trfrag(il, 6144 + ks * 1024); }
    f4 t[3] = {{0, 0, 0, 0}, {0, 0, 0, 0}, {0, 0, 0, 0}};
    if (MEM) {
        SB();
        asm volatile("s_waitcnt lgkmcnt(%0)\n\ts_barrier" :: "n"(4 * MPRE) : "memory");
#pragma unroll
        for (int ks = 0; ks < 3; ++ks) {
            q[ks] = trfrag(fl, ks * 1024);
            if (ks >= MPRE && ks < PF) { k[ks][0] = trfrag(il, ks * 1024); k[ks][1] = trfrag(il, 6144 + ks * 1024); }
            SB();
        }
        SB();
    }
    const int l = lane_id(), g = l >> 4, cc = l & 15;
    const u32 qo = L_IMG + 6144 - L_F + (c.wave & 1) * 16;
    const TB ql = TB{fl.a + qo, fl.b + qo};
    const u32 po = MEM ? mpb(c, s) : pxb(c, s) + (c.wave & 1) * 128;
    const u32 o1 = MEM ? po + MP_PX + io((c.h >> 1) * 1024) + (c.h & 1) * 128 : po + io(c.h * 2048 + 1024);
    const u32 o2 = MEM ? po + MP_PM + c.h * 128 : po + 131072 + io((c.h >> 2) * 2048 + 1024) + (c.h & 3) * 256;
    const u32 v1 = opq(g * 256 + (cc & 7) * 16 + (cc & 8)), v2 = opq(cc < 8 ? cc * 16 + (g & 1) * 8 + (g >> 1) * 4 : 0x80000000u);
    asm volatile("" :: "s"(o1), "s"(o2));
    if (MEM) {
        __builtin_amdgcn_s_setprio(0);
        SB();
#pragma unroll
        for (int ks = 0; ks < 6; ++ks) {
            t[1] = mfma(k[ks][0], q[ks], t[1]);
            t[2] = mfma(k[ks][1], q[ks], t[2]);
            SB();
            if (ks + PF < 6) { k[ks + PF][0] = trfrag(il, (ks + PF) * 1024); k[ks + PF][1] = trfrag(il, 6144 + (ks + PF) * 1024); }
            if (ks < 3) q[ks + 3] = trfrag(fl, (ks + 3) * 1024);
            SB();
        }
        __builtin_amdgcn_s_setprio(3);
        bar();
        TS(c, 2);
#pragma unroll
        for (int ks = 0; ks < 6; ++ks) { m[ks] = trfrag(fl, ks * 1024 + 16); SB(); }
    }
    float v[5], mx;
    if (!MEM) {
        bar();
        TS(c, 2);
        q[0] = trfrag(ql, 0); q[1] = trfrag(ql, 1024); m[0] = trfrag(fl, 16);
        SB();
#pragma unroll
        for (int ks = 0; ks < 6; ++ks) {
            t[1] = mfma(k[ks][0], q[ks], t[1]);
            SB();
            if (ks < 5) m[ks + 1] = trfrag(fl, (ks + 1) * 1024 + 16);
            SB();
            t[2] = mfma(k[ks][1], q[ks], t[2]);
            SB();
            if (ks + PF < 6) { k[ks + PF][0] = trfrag(il, (ks + PF) * 1024); k[ks + PF][1] = trfrag(il, 6144 + (ks + PF) * 1024); }
            SB();
            t[0] = mfma(m[ks], q[ks], t[0]);
            SB();
            if (ks < 4) q[ks + 2] = trfrag(ql, (ks + 2) * 1024);
            SB();
        }
    }
    const float sc = 0.07216878364870322f * 1.4426950408889634f;
#pragma unroll
    for (int i = 0; i < 4; ++i) {
        v[i] = cc < 8 ? t[1][i] : t[2][i];
        if (!MEM) v[i] = (4 * g + i + 8 > (c.wave & 1) * 8 + cc && cc >= 8) ? __uint_as_float(opq(0xFF800000u)) : v[i];
    }
    mx = opqf(fmx(fmx(v[0], v[1]), fmx(v[2], v[3])));
    if (MEM) {
        SB();
#pragma unroll
        for (int ks = 0; ks < 6; ++ks) t[0] = mfma(m[ks], q[ks], t[0]);
        SB();
    }
    v[4] = g < 2 ? (cc < 8 ? t[0][0] : t[0][1]) : (cc < 8 ? t[0][2] : t[0][3]);
    mx = fmx(mx, v[4]);
    mx = xmax(fmx(mx, ror8(mx))) * sc;
#pragma unroll
    for (int i = 0; i < 5; ++i) v[i] = __builtin_amdgcn_exp2f(__builtin_fmaf(v[i], sc, -mx));
    float sum = (v[0] + v[1]) + (v[2] + v[3]) + v[4];
    sum = xsum(sum + ror8(sum));
    const float inv = __builtin_amdgcn_rcpf(sum);
    gst64(c, o1, v1, v2u{pkc(v[0] * inv, v[1] * inv), pkc(v[2] * inv, v[3] * inv)});
    const float w = v[4] * inv;
    gst32(c, o2, v2, pkc(w, ror8(w)));
    __builtin_amdgcn_s_setprio(0);
}

DI void attn16(Ctx& c, int s) {
    bar();
    const int l = lane_id(), g = l >> 4, cc = l & 15;
    const TB il = mktb(L_IMG + g * 256 + rowp(l));
    f4 t[2] = {{0, 0, 0, 0}, {0, 0, 0, 0}};
    v4u q[6], k[6], m[6];
#pragma unroll
    for (int ks = 0; ks < 4; ++ks) { q[ks] = trfrag(il, ks * 1024); k[ks] = trfrag(il, 12288 + ks * 1024); }
    bar();
    TS(c, 2);
    m[0] = trfrag(il, L_F - L_IMG); m[1] = trfrag(il, L_F - L_IMG + 1024);
    SB();
#pragma unroll
    for (int ks = 0; ks < 6; ++ks) {
        t[1] = mfma(k[ks], q[ks], t[1]); t[0] = mfma(m[ks], q[ks], t[0]);
        SB();
        if (ks < 4) m[ks + 2] = trfrag(il, L_F - L_IMG + (ks + 2) * 1024);
        if (ks < 2) { q[ks + 4] = trfrag(il, (ks + 4) * 1024); k[ks + 4] = trfrag(il, 12288 + (ks + 4) * 1024); }
        SB();
    }
    const float sc = 0.07216878364870322f * 1.4426950408889634f, ninf = __uint_as_float(opq(0xFF800000u));
    float mx = ninf;
#pragma unroll
    for (int k = 0; k < 2; ++k)
#pragma unroll
        for (int i = 0; i < 4; ++i) {
            const bool vis = k == 0 ? g >= 2 : (4 * g + i <= cc);
            t[k][i] = vis ? t[k][i] : ninf; mx = fmx(mx, t[k][i]);
        }
    mx = xmax(mx) * sc;
    float sum = 0;
#pragma unroll
    for (int k = 0; k < 2; ++k)
#pragma unroll
        for (int i = 0; i < 4; ++i) { t[k][i] = __builtin_amdgcn_exp2f(__builtin_fmaf(t[k][i], sc, -mx)); sum += t[k][i]; }
    sum = xsum(sum);
    const float inv = __builtin_amdgcn_rcpf(sum);
    const v4u px = {pk(t[1][0] * inv, t[1][1] * inv), pk(t[1][2] * inv, t[1][3] * inv), 0u, 0u};
    const v2u pm = {pk(t[0][0] * inv, t[0][1] * inv), pk(t[0][2] * inv, t[0][3] * inv)};
    const u32 po = pxb(c, s);
    gst(c, po + io(c.h * 2048), l * 16, px);
    if (g >= 2) gst64(c, po + 131072 + io((c.h >> 2) * 2048) + (c.h & 3) * 256, cc * 16 + (g & 1) * 8, pm);
    __builtin_amdgcn_s_setprio(0);
}
DI void load_x_main(const Ctx& c, int s) {
    const int pw = (c.wave >> 1) ^ 2;
    load_x<1, 1>(c, s, 16 + pw, c.wave & 1, L_XH);
    load_x<2, 1>(c, s, 2 * pw, c.wave & 1, L_XL);
}

DI void qk_main(Ctx& c, const u16* wq, const u16* wk) {
    v8u W[24];
    if (c.par == 0 && c.p == 1 && c.h < NPAGE && c.wave == 0) probe_prep(c, c.h);
    load_x<2, 4>(c, c.p, 8 + (c.wave >> 1), c.wave & 1, L_XL);
    load_weights<false>(c, c.wave < 4 ? wq : wk, W);
    if (c.par == 0 && c.p == 1 && c.h < NPAGE) { if (c.wave == 0) probe_page(c, c.h); bar(); }
    load_x_main(c, c.p);
    barv();
    for (int s = c.p - 2; s < NSEG; s += 2) {
        asm volatile("" : "+s"(c.wave), "+s"(c.h), "+s"(c.par));
        if (s >= 0) {
            const int l = lane_id(), g = l >> 4, cc = l & 15;
            const u32 fa = L_F + ((c.wave & 3) * 48 + (cc ^ (4 * (cc >> 3)))) * 32 + (c.wave >> 2) * 16 + (g & 1) * 8;
            if (c.wave == 0) __builtin_amdgcn_s_setprio(3);
            else if (c.wave < 4) __builtin_amdgcn_s_setprio(2);
            else __builtin_amdgcn_s_setprio(1);
            const u32 pa = crit_gemm1<L_MEM>(c, s, W, l, fa + (g >> 1) * 512, fa + 1024 + (g >> 1) * 4, mem_in(c, s), [&](int i) {
                if (i == QB) asm volatile("s_cmp_lt_u32 %0, 4\n\ts_cbranch_scc1 1f\n\ts_barrier\n1:" :: "s"(c.wave) : "memory", "scc");
            });
            if (c.wave == 0) attn8<true>(c, s, pa);
            else if (c.wave == 1) attn16(c, s);
            else if (c.wave < 4) attn8<false>(c, s, pa);
            else {
                bar();
                __builtin_amdgcn_s_setprio(0);
                TS(c, 2);
                if (c.wave >= 6 && s + 2 < NSEG) load_x<5, 1>(c, s + 2, 8, c.wave & 1, L_XL);
                if (c.wave == 4) __builtin_amdgcn_s_sleep(QSLEEP);
            }
            TS(c, 3);
            if (s < 2) {
                const v4u sv = sentv();
                for (int i = c.wave * 64 + lane_id(); i < 30 * 40; i += 512) {
                    const int st = c.p + 4 + 2 * (i / 40), e = i % 40;
                    const u32 o = e < 32 ? MP_PX + io((c.h >> 1) * 1024) + ((e >> 3) * 16 + (c.h & 1) * 8 + (e & 7)) * 16 : MP_PM + c.h * 128 + (e & 7) * 16;
                    gst(c, 0, mpb(c, st) + o, sv);
                }
            }
        }
        auto flag_p = [&] { gst32(c, fb(c, s < 0 ? 0 : s) + 512 + c.h * 4, c.wave == 7 && s >= 0 ? 0u : 0x80000000u, opq(1u)); };
        if (s + 2 < NSEG) {
            f4 a[2];
            if (c.wave < 4) {
                bar();
                TS1(c, 5);
                if (c.wave < 2 && s >= 0) load_x<3, 1>(c, s + 2, 13, c.wave & 1, L_XL);
                if (s >= 2) {
                    out_part(c, s - 2, c.h, a, [] { asm volatile("s_waitcnt vmcnt(12)\n\ts_barrier" ::: "memory"); });
                    const u32 pa = L_IMG + c.wave * 2048 + lane_id() * 16;
                    st128(pa, __builtin_bit_cast(v4u, a[0])); st128(pa + 1024, __builtin_bit_cast(v4u, a[1]));
                } else barv();
                TS1(c, 6);
            }
            bulk_gemm1<false>(c, W, s + 2, [&](int q) {
                if (q == QM1) bar_half<true, true>(c.wave);
                if (q == QM0) bar_half<false, true>(c.wave);
                if (q == 15) bar_half<false, false>(c.wave);
                if (q == 20) { TS1(c, 7); bar_half<true, false>(c.wave); }
            });
            if (c.wave >= 4) {
                TS1(c, 5);
                if (s >= 2) {
                    out_part(c, s - 2, c.h, a, [&] { asm volatile("s_barrier" ::: "memory"); flag_p(); });
                    const u32 l16 = lane_id() * 16, pa = L_IMG + (c.wave - 4) * 2048 + l16;
                    a[0] += __builtin_bit_cast(f4, lds128(pa)); a[1] += __builtin_bit_cast(f4, lds128(pa + 1024));
                    const u32 pb = L_X + (c.wave >= 6 ? c.wave - 2 : 0) * 2048 + l16;
                    if (c.wave != 4) st128(pb, __builtin_bit_cast(v4u, a[0]));
                    if (c.wave != 5) st128(pb + 1024, __builtin_bit_cast(v4u, a[1]));
                    TS1(c, 6);
                    bar();
                    if (c.wave < 6) {
                        const u32 pc = L_X + (c.wave & 1) * 1024 + l16;
                        f4 r = (c.wave & 1) ? a[1] : a[0];
                        r += __builtin_bit_cast(f4, lds128(pc));
                        r += __builtin_bit_cast(f4, lds128(pc + 8192)) + __builtin_bit_cast(f4, lds128(pc + 10240));
                        out_store(c, s - 2, c.h, c.wave & 1, r);
                    }
                } else { bar(); flag_p(); bar(); }
            }
            TS(c, 4);
            if (s + 4 < NSEG) load_x_main(c, s + 4);
            if (s < 0) get_mask(c);
            else flag_wait(c, s, false, 1);
        } else {
            barv(); flag_p();
            for (int t = s - 2; t <= s; t += 2) { flag_wait(c, t, false, 1); out_duty<L_MEM>(c, t, c.h, [] {}); }
        }
        TSF(c, s);
    }
}

DI u32 wo_off(int i, int ks) { return ((i - 8) * 2 + ks / 3) * 24576 + (ks % 3) * 4096; }
DI u32 z_lane(int l) { const int g = l >> 4; return ((g >> 1) + 2 * (g & 1)) * 65536 + (l & 15) * 16; }
DI void swap32(f4& a, f4& b, int i) {
    auto s = __builtin_amdgcn_permlane32_swap(__float_as_uint(a[i]), __float_as_uint(b[i]), false, false);
    a[i] = __uint_as_float(s[0]); b[i] = __uint_as_float(s[1]);
}
DI v4u z_pack(const u32 (&x)[2], const u32 (&y)[2]) {
    auto s0 = __builtin_amdgcn_permlane16_swap(x[0], y[0], false, false);
    auto s1 = __builtin_amdgcn_permlane16_swap(x[1], y[1], false, false);
    return v4u{s0[0], s1[0], s0[1], s1[1]};
}
template <class F> DI void z_tail(Ctx& c, u32 zo, TB wl, TB wh, TB il, F mid) {
    v4u a[3]; v8u T[2][3];
    asm volatile("" ::: "memory");
    SB();
    T[0][0] = cat8(trfrag(wl, wo_off(8, 0)), trfrag(wl, wo_off(8, 1))); T[1][0] = cat8(trfrag(wh, wo_off(8, 0)), trfrag(wh, wo_off(8, 1)));
    asm volatile("s_waitcnt lgkmcnt(8)\n\ts_barrier" ::: "memory");
    TS(c, 2);
#pragma unroll
    for (int f = 0; f < 3; ++f) a[f] = trfrag(il, f * 1024);
    SB();
    const int l = lane_id(), g = l >> 4;
    const u32 idx = 0x4444u + (l & 8) * 0x1555u + ((l & 8) >> 2);
    f4 acc[2] = {{0, 0, 0, 0}, {0, 0, 0, 0}};
    mid();
    SB();
#pragma unroll
    for (int f = 0; f < 3; ++f)
#pragma unroll
        for (int j = 0; j < 2; ++j) {
            acc[j] = smf(a[f], T[j][f], acc[j], idx);
            SB();
            if (f < 2) T[j][f + 1] = cat8(trfrag(j ? wh : wl, wo_off(8, 2 * f + 2)), trfrag(j ? wh : wl, wo_off(8, 2 * f + 3)));
            SB();
        }
    const u32 x[2] = {pkc(fold32(acc[0][0], acc[1][0]), fold32(acc[0][1], acc[1][1])), pkc(fold32(acc[0][2], acc[1][2]), fold32(acc[0][3], acc[1][3]))};
    gst(c, zo, (g & 1) ? 0x80000000u : z_lane(l), z_pack(x, x));
    __builtin_amdgcn_s_setprio(0);
}

DI void v_main(Ctx& c, const u16* wv) {
    v8u W[24];
    load_weights<false>(c, wv, W);
    NodeA na = {};
    for (int s = c.p - 4; s < NSEG; s += 2) {
        asm volatile("" : "+s"(c.wave), "+s"(c.h), "+s"(c.par));
        if (s >= 0) {
            const int l = lane_id(), g = l >> 4, cc = l & 15;
            const int rho = ((cc >> 2) << 1) | (cc & 1);
            const u32 a = V_VS + ((cc >> 1) & 1) * 16 + (g & 1) * 8;
            const int t0 = c.wave * 3 + (g >> 1), t2 = c.wave * 3 + 2;
            TB wl, wh, il;
            const u32 pa = opq((12288 + g * 1024 + c.wave * 256 + rowp(l)) | (g * 256 + rowp(l)) << 16);
            __builtin_amdgcn_s_setprio(VPRIO);
            crit_gemm1<V_MEM>(c, s, W, l, a + (t0 * 8 + (rho ^ (4 * (t0 & 1)))) * 32, a + (t2 * 8 + (rho ^ (4 * (t2 & 1)))) * 32 + (g >> 1) * 4,
                na.mi, [&](int i) {
                    if (i == 7) { wl = mktb(pa & 0xFFFFu); il.a = opq(V_VS + (pa >> 16)); }
                    if (i == 8) { il.b = opq(il.a ^ 128u); wh = TB{opq(wl.a + 49152), opq(wl.b + 49152)}; }
                });
            v4u u[2][4];
            z_tail(c, na.zo + 2 * 262144, wl, wh, il, [&] { node_u<0, VPRE>(c, na, u); });
            TS(c, 6);
            if (out_vm(c) && s >= 2) {
                f4 a[2];
                __builtin_amdgcn_s_sleep(OVSLEEP);
                out_part(c, s - 2, c.h + 12, a, [] {});
                const u32 pa = V_MEM + c.wave * 2048 + lane_id() * 16;
                st128(pa, __builtin_bit_cast(v4u, a[0])); st128(pa + 1024, __builtin_bit_cast(v4u, a[1]));
                TS(c, 5);
            } else if (c.h >= 20) { if (c.h >= 28 && s + 8 < NSEG) { prefetch_x(c, s + 8, V_DUMP); prefetch_x(c, s + 8, V_DUMP, 4); } }
            else if (s < NSEG - 1) { node_u<VPRE, 8>(c, na, u); node_duty<NSLEEP + NVX>(c, na, u); }
            if (c.h < 20 && s < 2) {
                const v4u sv = sentv();
                for (int i = c.wave * 64 + lane_id(); i < 30 * 32; i += 256) {
                    const int st = 5 + c.p + 2 * (i >> 5);
                    if (st < NSEG) gst(c, 0, mpb(c, st) + io(c.h * 512) + (i & 31) * 16, sv);
                }
            }
        } else {
            if (s >= -2) get_mask(c);
            TS(c, 6);
            barv();
            TS(c, 5);
        }
        if (s + 4 < NSEG) bulk_gemm1<true>(c, W, s + 4, [&](int q) {
            if (q == 9) na = node_addr(c, (s + 2) & 63);
            if (q == 20) { TS(c, 3); bar(); }
        });
        else { na = node_addr(c, (s + 2) & 63); bar(); }
        if (out_vm(c) && s >= 2) bar();
        TS(c, 4);
        vm_duty(c, s);
        TSF(c, s);
    }
}

DI void bulk_gemm2(const Ctx& c, const v8u (&W)[24], int s) {
    const int l = lane_id();
    const TB wl = wbase<true>(c, l), il = mktb(L_IMG + (l >> 4) * 256 + rowp(l));
    const u32 uo = ub(c, s, c.wave & 3) + io(c.h * 1024);
    v4u a[20][2], b[20];
#pragma unroll
    for (int i = 0; i < 4; ++i) a[i >> 1][i & 1] = trfrag(il, (i & 1) * 6144 + (i >> 1) * 1024);
#pragma unroll
    for (int ig = 0; ig < 3; ++ig) {
        const int nt = ig == 0 ? 4 : 3;
        f4 acc[2][4];
#pragma unroll
        for (int j = 0; j < 4; ++j) { acc[0][j] = f4{0, 0, 0, 0}; acc[1][j] = f4{0, 0, 0, 0}; }
#pragma unroll
        for (int ks = 0; ks < 6; ++ks) {
            const int st = ig * 6 + ks;
#pragma unroll
            for (int m = 0; m < 2; ++m) {
                SB();
#pragma unroll
                for (int j = 0; j < nt; ++j) {
                    const int i = ig == 0 ? j : (j < 2 ? 2 + 2 * ig + j : 7 + ig);
                    acc[m][j] = mfma(a[st][m], i < 8 ? ((ks & 1) ? hi4(W[i * 3 + (ks >> 1)]) : lo4(W[i * 3 + (ks >> 1)])) : b[st], acc[m][j]);
                    if (m == 0 && j == 0 && st >= 5 && st < 17) { SB(); b[st + 1] = trfrag(wl, wo_off(7 + (st + 1) / 6, (st + 1) % 6)); SB(); }
                }
                SB();
                if (st < 16) a[st + 2][m] = trfrag(il, m * 6144 + ((st + 2) % 6) * 1024);
            }
        }
        SB();
#pragma unroll
        for (int j = 0; j < nt; ++j) {
            const int i = ig == 0 ? j : (j < 2 ? 2 + 2 * ig + j : 7 + ig);
            const v4u v = {pk(acc[0][j][0], acc[0][j][1]), pk(acc[0][j][2], acc[0][j][3]), pk(acc[1][j][0], acc[1][j][1]), pk(acc[1][j][2], acc[1][j][3])};
            gst(c, uo + i * 65536, l * 16, v);
        }
        SB();
    }
}

DI void o_main(Ctx& c, const u16* wo) {
    v8u W[24];
    load_weights<true>(c, wo, W);
    load_x<2, 2>(c, c.p, 16 + ((c.wave >> 1) & 1), c.wave & 1, V_XH);
    load_x<4, 2>(c, c.p, (c.wave >> 1) & 1, c.wave & 1, V_XL);
    for (int s = c.p - 4; s < NSEG; s += 2) {
        asm volatile("" : "+s"(c.wave), "+s"(c.h), "+s"(c.par));
        if (s >= 0) {
            const int l = lane_id(), g = l >> 4;
            const TB il = mktb(V_VS + g * 256 + rowp(l));
            const u32 idx = opq(0x4444u + (l & 8) * 0x1555u + ((l & 8) >> 2)), za = opq(z_lane(l));
            const NodeA na = node_addr(c, s);
            u32 zo = na.zo, zo1 = zo + 262144;
            asm volatile("" : "+s"(zo1));
            stage_mem<V_MEM>(c, s, na.mi);
            v4u u[2][4];
            if (c.h < 20) flag_wait(c, s, true, 1);
            node_u<0, UPRE>(c, na, u);
            f4 acc[8];
#pragma unroll
            for (int i = 0; i < 4; ++i) { acc[i] = f4{0, 0, 0, 0}; asm volatile("" : "+v"(acc[i])); }
            __builtin_amdgcn_s_setprio(OPRIO);
            TS2(c, 6);
            asm volatile("s_waitcnt vmcnt(%0) lgkmcnt(0)\n\ts_barrier" :: "n"(UPRE) : "memory");
            TS(c, 2);
            v4u a[3];
#pragma unroll
            for (int f = 0; f < 3; ++f) a[f] = trfrag(il, f * 1024);
            SB();
            u32 xp[4][2];
#pragma unroll
            for (int pr = 0; pr < 5; ++pr) {
                float x[4];
#pragma unroll
                for (int n = 0; n < 6; ++n) {
                    if (pr < 4) acc[2 * pr + (n & 1)] = smf(a[n >> 1], W[(2 * pr + (n & 1)) * 3 + (n >> 1)], acc[2 * pr + (n & 1)], idx);
                    SB();
                    if (pr > 0) {
                        f4& p = acc[2 * pr - 2]; f4& q = acc[2 * pr - 1];
                        if (n == 1) { swap32(p, q, 0); swap32(p, q, 1); }
                        if (n == 2) { swap32(p, q, 2); swap32(p, q, 3); x[0] = opqf(p[0] + q[0]); }
                        if (n == 3) { x[1] = opqf(p[1] + q[1]); x[2] = opqf(p[2] + q[2]); x[3] = opqf(p[3] + q[3]); }
                        if (n == 4) { xp[pr - 1][0] = pkc(x[0], x[1]); xp[pr - 1][1] = pkc(x[2], x[3]); }
                        if (n == 5 && !(pr & 1)) gst(c, pr == 2 ? zo : zo1, za, z_pack(xp[pr - 2], xp[pr - 1]));
                        if (n >= 4 && pr < 3) { f4& z = acc[2 * pr + n - 2]; z = f4{0, 0, 0, 0}; asm volatile("" : "+v"(z)); }
                    }
                    SB();
                }
            }
            __builtin_amdgcn_s_setprio(0);
            TS(c, 3);
            if (c.h < 20 && s < NSEG - 1) { node_u<UPRE, 8>(c, na, u); node_duty<NSLEEP>(c, na, u); }
        } else {
            if (s >= -2) get_mask(c);
            TS(c, 6);
            barv();
            TS(c, 5);
        }
        const bool bulk = s >= -2 && s + 2 < NSEG;
        if (bulk) {
            __builtin_amdgcn_s_setprio(UPRIO);
            bulk_gemm2(c, W, s + 2);
            __builtin_amdgcn_s_setprio(0);
            const int l = lane_id(), g = l >> 4;
            const v4u sv = sentv();
            const u32 za = z_lane(l);
#pragma unroll
            for (int k = 0; k < 3; ++k)
                if (k < 2 || !(g & 1)) gst(c, zb(c, s + 2) + k * 262144 + (c.wave & 3) * 16384 + c.h * 256, za, sv);
        }
        auto flag_u = [&] { if (bulk) { asm volatile("s_waitcnt vmcnt(0)" ::: "memory"); gst32(c, fb(c, s + 2) + (c.wave & 3) * 128 + c.h * 4, 0, opq(1u)); } };
        if (out_vm(c) && s >= 2) {
            f4 a[2];
            TS(c, 5);
            out_part(c, s - 2, c.h + 12, a, [] {});
            flag_u();
            TS(c, 0);
            bar();
            TS(c, 1);
            const u32 l16 = lane_id() * 16, pa = V_MEM + (c.wave - 4) * 2048 + l16;
            a[0] += __builtin_bit_cast(f4, lds128(pa)); a[1] += __builtin_bit_cast(f4, lds128(pa + 1024));
            const u32 pb = V_X + 4096 + (c.wave >= 6 ? c.wave - 5 : 0) * 8192 + l16;
            if (c.wave != 4) st128(pb, __builtin_bit_cast(v4u, a[0]));
            if (c.wave != 5) st128(pb + 1024, __builtin_bit_cast(v4u, a[1]));
            bar();
            if (c.wave < 6) {
                const u32 pc = V_X + 4096 + (c.wave & 1) * 1024 + l16;
                f4 r = (c.wave & 1) ? a[1] : a[0];
                r += __builtin_bit_cast(f4, lds128(pc));
                r += __builtin_bit_cast(f4, lds128(pc + 8192)) + __builtin_bit_cast(f4, lds128(pc + 16384));
                out_store(c, s - 2, c.h + 12, c.wave & 1, r);
            }
        } else {
            TS(c, 0);
            bar();
            TS(c, 1);
            flag_u();
        }
        TS(c, 4);
        vm_duty(c, s);
        TSF(c, s);
    }
}

__global__ void __launch_bounds__(512, 2) memtr_kernel(Args a) {
    Ctx c;
    const int wg = blockIdx.x;
    c.wave = __builtin_amdgcn_readfirstlane(threadIdx.x >> 6);
    const int br = (wg & 7) >> 2;
    const int idx = (wg >> 3) * 4 + (wg & 3);
    c.h = idx & 31; c.p = idx >> 6;
    c.r = __builtin_amdgcn_make_buffer_rsrc(a.ws, 0, WS_SPAN, 0x00020000);
    c.par = br ? ~0u : 0u;
    c.spin = 0;
    c.x = br ? a.x[1] : a.x[0]; c.mem0 = br ? a.mem0[1] : a.mem0[0]; c.out = a.out + br * 640;
    if (((idx >> 5) & 1) == 0) qk_main(c, br ? a.w[1][0] : a.w[0][0], br ? a.w[1][1] : a.w[0][1]);
    else {
        if (c.h >= 16) prefetch_x(c, c.p + ((c.h >> 2) & 3) * 2, V_XL + 7 * 2048);
        else if (c.h >= 12 && c.wave < 5) {
            const unsigned char* m = (const unsigned char*)c.mem0 + c.wave * 2048;
            dmai<0, 0>(m, lane_id() * 16, V_XL + 7 * 2048); dmai<1024, 0>(m, lane_id() * 16, V_XL + 7 * 2048);
        }
        if (c.wave < 7) { load_x<1, 1>(c, c.p, 8 + c.wave, 0, V_XL); load_x<1, 1>(c, c.p, 8 + c.wave, 1, V_XL); }
        if (c.wave < 4) v_main(c, br ? a.w[1][2] : a.w[0][2]);
        else o_main(c, br ? a.w[1][3] : a.w[0][3]);
    }
}

extern "C" void kernel_launch(void* const* d_in, const int* in_sizes, int n_in, void* d_out, int out_size, void* d_ws, size_t ws_size, hipStream_t stream) {
    Args a;
    for (int b = 0; b < 2; ++b) {
        a.x[b] = (const u16*)d_in[7 * b]; a.mem0[b] = (const u16*)d_in[7 * b + 1];
        for (int k = 0; k < 4; ++k) a.w[b][k] = (const u16*)d_in[7 * b + 2 + k];
    }
    a.out = (u16*)d_out; a.ws = (unsigned char*)d_ws;
    (void)hipMemsetAsync((unsigned char*)d_ws + INIT_OFF, 0xFF, INIT_BYTES, stream);
    memtr_kernel<<<256, 512, 0, stream>>>(a);
}
```

```cpp
#include <hip/hip_runtime.h>
#include <stdint.h>

typedef unsigned u32;
typedef unsigned short u16;
typedef u32 v2u __attribute__((ext_vector_type(2)));
typedef u32 v4u __attribute__((ext_vector_type(4)));
typedef u32 v8u __attribute__((ext_vector_type(8)));
typedef float f4 __attribute__((ext_vector_type(4)));
typedef float f2 __attribute__((ext_vector_type(2)));
typedef short s4 __attribute__((ext_vector_type(4)));
typedef __bf16 b2 __attribute__((ext_vector_type(2)));
typedef __bf16 b8 __attribute__((ext_vector_type(8)));
typedef __bf16 b16 __attribute__((ext_vector_type(16)));
#define AS3 __attribute__((address_space(3)))
#define DI __device__ __forceinline__

constexpr int NSEG = 64;
constexpr u32 SENT = 0xFFFFFFFFu;
constexpr int NPAGE = 17;
constexpr u32 WS_SPAN = NPAGE * 2097152;
constexpr u32 INIT_OFF = 2097152 - 147456, INIT_BYTES = 147456 + 5 * 65536;
constexpr u32 MP_PM = 20480, MP_PX = 32768;
constexpr u32 L_IMG = 98304, L_X = 122880, L_MEM = L_X + 24576, L_F = L_X + 34816;
constexpr u32 V_MEM = L_IMG + 12288, V_VS = V_MEM + 10240, V_DUMP = V_VS + 3072, V_X = V_DUMP + 1024;
#define QSLEEP 5
#define UPRIO 1
#define QM0 7
#define QM1 1
#define NSLEEP 4
#define OSLEEP 0
#define OVSLEEP 0
#define UPRE 6
#define VPRE 4
#define VPRIO 3
#define OPRIO 3
#define PSLEEP 3
#ifndef NVX
#define NVX 4
#endif
#define VSLEEP 0
constexpr u32 SPIN_MAX = 1u << 20;

struct Args { const u16* x[2]; const u16* mem0[2]; const u16* w[2][4]; u16* out; unsigned char* ws; };

__shared__ __attribute__((aligned(1024))) unsigned char lds[163840];

DI v4u lds128(u32 a) { return *(AS3 v4u*)((AS3 unsigned char*)lds + a); }
DI f2 lds64f(u32 a) { return *(AS3 f2*)((AS3 unsigned char*)lds + a); }
DI void st128(u32 a, v4u v) { *(AS3 v4u*)((AS3 unsigned char*)lds + a) = v; }
DI void st64(u32 a, v2u v) { *(AS3 v2u*)((AS3 unsigned char*)lds + a) = v; }
DI void st32(u32 a, u32 v) { *(AS3 u32*)((AS3 unsigned char*)lds + a) = v; }
DI v2u ldtr(u32 a) { return __builtin_bit_cast(v2u, __builtin_amdgcn_ds_read_tr16_b64_v4i16((AS3 s4*)((AS3 unsigned char*)lds + a))); }
struct TB { u32 a, b; };
DI u32 opq(u32 v);
DI TB mktb(u32 a) { TB t = {opq(a), opq(a ^ 128u)}; return t; }
DI v4u trfrag(TB t, u32 off) { v2u lo = ldtr(t.a + off), hi = ldtr(t.b + off); v4u r = {lo.x, lo.y, hi.x, hi.y}; return r; }
DI int lane_id() { int l; asm volatile("v_mbcnt_lo_u32_b32 %0, -1, 0\n\tv_mbcnt_hi_u32_b32 %0, -1, %0" : "=v"(l)); return l; }
DI u32 opq(u32 v) { asm volatile("" : "+v"(v)); return v; }
DI float opqf(float v) { asm volatile("" : "+v"(v)); return v; }
DI v4u sentv() { const u32 v = opq(SENT); v4u r = {v, v, v, v}; return r; }
DI u32 rowp(int l) { return ((((l & 15) >> 2) ^ ((l >> 2) & 4)) * 32 + (l & 3) * 8); }
DI v8u cat8(v4u a, v4u b) { return __builtin_shufflevector(a, b, 0, 1, 2, 3, 4, 5, 6, 7); }
DI v4u lo4(v8u a) { return __builtin_shufflevector(a, a, 0, 1, 2, 3); }
DI v4u hi4(v8u a) { return __builtin_shufflevector(a, a, 4, 5, 6, 7); }
DI f4 mfma(v4u a, v4u b, f4 c) { return __builtin_amdgcn_mfma_f32_16x16x32_bf16(__builtin_bit_cast(b8, a), __builtin_bit_cast(b8, b), c, 0, 0, 0); }
DI f4 smf(v4u a, v8u b, f4 c, u32 idx) { return __builtin_amdgcn_smfmac_f32_16x16x64_bf16(__builtin_bit_cast(b8, a), __builtin_bit_cast(b16, b), c, (int)idx, 0, 0); }
DI u32 pk(float a, float b) { b2 r = {(__bf16)a, (__bf16)b}; return __builtin_bit_cast(u32, r); }
DI u32 pkc(float a, float b) { u32 r = pk(a, b); return r < 0xFFFFFFFEu ? r : 0xFFFFFFFEu; }
DI void bar() { asm volatile("s_waitcnt lgkmcnt(0)\n\ts_barrier" ::: "memory"); }
DI void barv() { asm volatile("s_waitcnt vmcnt(0) lgkmcnt(0)\n\ts_barrier" ::: "memory"); }
template <bool LOW, bool VMEM> DI void bar_half(int wave) {
    if (VMEM) asm volatile("s_cmp_lt_u32 %0, 4\n\ts_cbranch_scc%1 LBH%=\n\ts_waitcnt vmcnt(0) lgkmcnt(0)\n\ts_barrier\nLBH%=:" :: "s"(wave), "n"(LOW ? 0 : 1) : "memory", "scc");
    else asm volatile("s_cmp_lt_u32 %0, 4\n\ts_cbranch_scc%1 LBH%=\n\ts_waitcnt lgkmcnt(0)\n\ts_barrier\nLBH%=:" :: "s"(wave), "n"(LOW ? 0 : 1) : "memory", "scc");
}
#define WHINT " nt"
#define PROUNDS 3
#define ASLEEP 1
#define KSLEEP 0
#define QB 7
#define MPF 3
#define MPRE 1
#define BSLEEP 0
#define OAUX 2
DI void dma(const void* sbase, u32 voff, u32 ldst) {
    asm volatile("s_mov_b32 m0, %2\n\ts_nop 0\n\tglobal_load_lds_dwordx4 %0, %1" WHINT :: "v"(voff), "s"(sbase), "s"(ldst) : "memory");
}
template <int GO, int LO> DI void dmai(const void* sbase, u32 voff, u32 ldst) {
    asm volatile("s_add_u32 m0, %2, %3\n\ts_nop 0\n\tglobal_load_lds_dwordx4 %0, %1 offset:%4" :: "v"(voff), "s"(sbase), "s"(ldst), "n"(LO - GO), "n"(GO) : "memory", "scc");
}
DI float fold32(float a, float b) { auto s = __builtin_amdgcn_permlane32_swap(__float_as_uint(a), __float_as_uint(b), false, false); return __uint_as_float(s[0]) + __uint_as_float(s[1]); }
DI float fmx(float a, float b) { return __builtin_elementwise_maximum(a, b); }
DI float xmax(float x) {
    auto s = __builtin_amdgcn_permlane16_swap(__float_as_uint(x), __float_as_uint(x), false, false); x = fmx(__uint_as_float(s[0]), __uint_as_float(s[1]));
    auto t = __builtin_amdgcn_permlane32_swap(__float_as_uint(x), __float_as_uint(x), false, false); return fmx(__uint_as_float(t[0]), __uint_as_float(t[1]));
}
DI float xsum(float x) {
    auto s = __builtin_amdgcn_permlane16_swap(__float_as_uint(x), __float_as_uint(x), false, false); x = __uint_as_float(s[0]) + __uint_as_float(s[1]);
    auto t = __builtin_amdgcn_permlane32_swap(__float_as_uint(x), __float_as_uint(x), false, false); return __uint_as_float(t[0]) + __uint_as_float(t[1]);
}
DI float ror8(float x) { return __uint_as_float(__builtin_amdgcn_update_dpp(0, __float_as_uint(x), 0x128, 0xF, 0xF, true)); }
DI u32 umax(u32 a, u32 b) { return a > b ? a : b; }
DI u32 max4(v4u v) { return umax(umax(v.x, v.y), umax(v.z, v.w)); }

struct Ctx {
    __amdgpu_buffer_rsrc_t r; u32 par; int h, p, wave; u32 spin;
    const u16* x; const u16* mem0; u16* out;
};
DI constexpr u32 io(u32 o) { return ((o >> 13) << 14) | (o & 8191u); }
DI u32 pgb(const Ctx& c, u32 page) { return (page << 21) | (((c.par >> page) & 1u) << 13); }
DI u32 mpb(const Ctx& c, int s) { return pgb(c, 1 + ((u32)s >> 5)) + ((u32)s & 31u) * 65536u; }
DI u32 zb(const Ctx& c, int s) { const u32 b = (u32)s & 7u, q = (b * 11u) >> 5; return pgb(c, 3 + q) + (b - 3u * q) * 655360u; }
DI u32 ub(const Ctx& c, int s, u32 w) { const u32 b = ((u32)s & 7u) * 4u + w, q = (b * 43u) >> 7; return pgb(c, 6 + q) + (b - 3u * q) * 655360u; }
DI u32 pxb(const Ctx& c, int s) { return pgb(c, 0) + ((u32)s % 6u) * 163840u; }
DI u32 fb(const Ctx& c, int s) { return pgb(c, 0) + (INIT_OFF + 16384) + io((u32)s * 1024u); }
DI v4u gld(const Ctx& c, u32 off, u32 voff) { return __builtin_amdgcn_raw_buffer_load_b128(c.r, voff, off, 16); }
DI void gst(const Ctx& c, u32 off, u32 voff, v4u v) { __builtin_amdgcn_raw_buffer_store_b128(v, c.r, voff, off, 16); }
DI void gst64(const Ctx& c, u32 off, u32 voff, v2u v) { __builtin_amdgcn_raw_buffer_store_b64(v, c.r, voff, off, 16); }
DI void gst32(const Ctx& c, u32 off, u32 voff, u32 v) { __builtin_amdgcn_raw_buffer_store_b32(v, c.r, voff, off, 16); }

#define TS(c, k)
#define TSF(c, s)
#define TS1(c, k) TS(c, k)
#define TS2(c, k)
#define SB() __builtin_amdgcn_sched_barrier(0)

template <int N> DI void poll(Ctx& c, const u32 (&off)[N], const u32 (&voff)[N], v4u (&v)[N]) {
    for (;;) {
        asm volatile("" ::: "memory");
        u32 m = 0;
#pragma unroll
        for (int k = 0; k < N; ++k) v[k] = gld(c, off[k], voff[k]);
#pragma unroll
        for (int k = 0; k < N; ++k) m = umax(m, max4(v[k]));
        if (__builtin_amdgcn_ballot_w64(m == SENT) == 0) return;
        if (++c.spin > SPIN_MAX) return;
    }
}

DI void probe_prep(const Ctx& c, int pg) {
    const int l = lane_id();
    __builtin_amdgcn_raw_buffer_store_b32(opq(SENT), c.r, (l & 1) * 8192 + ((l >> 1) & 3) * 256, pg * 2097152 + 1048576, 16);
}
DI void probe_page(const Ctx& c, int pg) {
    u32 best[2] = {~0u, ~0u};
    const u32 o = pg * 2097152 + 1048576;
    for (int r = 0; r < PROUNDS; ++r) {
#pragma unroll
        for (int k = 0; k < 2; ++k) {
            const u32 t0 = (u32)__builtin_amdgcn_s_memtime();
            const u32 v = __builtin_amdgcn_raw_buffer_load_b32(c.r, 0, o + k * 8192 + r * 256, 16);
            asm volatile("s_waitcnt vmcnt(0)" :: "v"(v) : "memory");
            const u32 d = (u32)__builtin_amdgcn_s_memtime() - t0;
            best[k] = d < best[k] ? d : best[k];
        }
    }
    __builtin_amdgcn_raw_buffer_store_b32(opq(best[1] < best[0] ? 1u : 0u), c.r, 0, INIT_OFF + pg * 4, 16);
}
DI void get_mask(Ctx& c) {
    const int l = lane_id();
    const u32 vo = (l < NPAGE ? l : 0) * 4;
    for (;;) {
        asm volatile("" ::: "memory");
        const u32 v = __builtin_amdgcn_raw_buffer_load_b32(c.r, vo, INIT_OFF, 16);
        if (__builtin_amdgcn_ballot_w64(v == SENT) == 0 || ++c.spin > SPIN_MAX) { c.par ^= (u32)__builtin_amdgcn_ballot_w64(v == 1u); return; }
    }
}

template <int N, int ST, int I = 0> DI void dma_x(const unsigned char* b, u32 voff, u32 dst) {
    if constexpr (I < N) { dmai<I * ST * 64, I * ST * 2048>(b, voff, dst); dma_x<N, ST, I + 1>(b, voff, dst); }
}
template <int N, int ST> DI void load_x(const Ctx& c, int s, int ks, int m, u32 dst) {
    const int l = lane_id();
    dma_x<N, ST>((const unsigned char*)c.x + (s * 40960 + m * 20480 + ks * 64), (l >> 2) * 1280 + (((l & 3) ^ (-(l >> 4) & 3)) << 4), dst + ks * 2048 + m * 1024);
}
DI u32 xfrag(int l) { return (l & 15) * 64 + (((l >> 4) ^ (-(l >> 2) & 3)) << 4); }
constexpr u32 V_XH = V_X - 16 * 2048, V_XL = V_X + 4 * 2048, L_XH = L_X - 16 * 2048, L_XL = L_X + 4 * 2048;
DI void prefetch_x(const Ctx& c, int s, u32 dump, int part = 0) {
    const unsigned char* b = (const unsigned char*)c.x + (s * 40960 + (c.wave + part) * 5120);
    const u32 voff = lane_id() * 16;
    dmai<0, 0>(b, voff, dump); dmai<1024, 0>(b, voff, dump); dmai<2048, 0>(b, voff, dump); dmai<3072, 0>(b, voff, dump); dmai<0, 0>(b + 4096, voff, dump);
}
DI u32 flag_load(const Ctx& c, int s, bool vm) {
    const int l = lane_id();
    const bool node = vm && c.h < 20;
    const u32 w = node ? (c.h & 1) * 2 : (c.h & 3), d = node ? 128 : 512 - w * 128;
    return __builtin_amdgcn_raw_buffer_load_b32(c.r, (l & 3) * 4 + ((l & 4) ? d : 0) + (node ? (l & 8) * 8 : 0), fb(c, s < 0 ? 0 : s) + w * 128 + (node ? c.wave & 3 : c.wave) * 16, 16);
}
DI u32 pending(u32 v) { u32 r = (u32)__builtin_amdgcn_ballot_w64(v == SENT); asm volatile("" : "+s"(r)); return r; }
DI void flag_wait(Ctx& c, int s, bool vm, u32 miss) {
    while (miss != 0 && ++c.spin < SPIN_MAX) { asm volatile("" ::: "memory"); miss = pending(flag_load(c, s, vm)); }
    asm volatile("" ::: "memory");
}

DI u32 mem_in(const Ctx& c, int s) { return mpb(c, s) + io((c.wave < 4 ? c.wave : 4) * 2048); }
template <u32 LM> DI void stage_mem(Ctx& c, int s, u32 o) {
    if (c.wave < 4 || c.wave == (LM == L_MEM ? 4 : 7)) {
        const int l = lane_id(), sl = c.wave < 4 ? c.wave : 4;
        v4u v[2];
        if (s == 0) {
            const __amdgpu_buffer_rsrc_t mr = __builtin_amdgcn_make_buffer_rsrc((void*)c.mem0, 0, 10240, 0x00020000);
            const u32 m = (l & 7) * 1280 + (l >> 4) * 32 + (l & 8) / 2;
#pragma unroll
            for (int k = 0; k < 2; ++k)
#pragma unroll
                for (int j = 0; j < 4; ++j) v[k][j] = __builtin_amdgcn_raw_buffer_load_b32(mr, m + k * 128 + j * 8, sl * 256, 0);
        } else {
            const u32 off[2] = {o, o}; const u32 voff[2] = {(u32)l * 16, (u32)l * 16 + 1024};
            poll<2>(c, off, voff, v);
        }
        st128(LM + sl * 2048 + l * 16, v[0]);
        st128(LM + sl * 2048 + 1024 + l * 16, v[1]);
    }
    if (LM == L_MEM) barv(); else bar();
}

struct NodeA { u32 u0, u1, px, pm, z, mo, mi, zo; };
DI NodeA node_addr(const Ctx& c, int t) {
    const u32 n = c.h, uo = (n >> 1) * 65536 + io(c.wave * 4096), po = mpb(c, t);
    NodeA a = {ub(c, t, (2 * n) & 3) + uo, ub(c, t, (2 * n + 1) & 3) + uo, po + MP_PX + io(c.wave * 2048), po + MP_PM + c.wave * 512,
               zb(c, t) + n * 32768 + c.wave * 1024, mpb(c, t + 1) + io((2 * n + (c.wave & 1)) * 256),
               po + io((c.wave < 4 ? c.wave : 4) * 2048), zb(c, t) + (c.wave & 3) * 16384 + n * 256};
    asm volatile("" : "+s"(a.u0), "+s"(a.u1), "+s"(a.px), "+s"(a.pm), "+s"(a.z), "+s"(a.mo), "+s"(a.mi), "+s"(a.zo));
    return a;
}
template <int I0, int I1> DI void node_u(const Ctx& c, const NodeA& na, v4u (&u)[2][4]) {
    const u32 l16 = lane_id() * 16 + (c.h < 20 ? 0u : 0x80000000u);
#pragma unroll
    for (int i = I0; i < I1; ++i) u[i >> 2][i & 3] = gld(c, i >> 2 ? na.u1 : na.u0, l16 + (i & 3) * 1024);
}
template <int SLP> DI void node_duty(Ctx& c, const NodeA& na, v4u (&u)[2][4]) {
    const int l = lane_id();
    const u32 l16 = l * 16;
    const u32 mo = na.mo;
    const u32 off[3] = {na.px, na.px, na.pm}, zoff[2] = {na.z, na.z + 16384};
    const u32 voff[3] = {l16, l16 + 1024, (u32)((l >> 4) * 128 + (l & 7) * 16)}, zvoff[2] = {l16, l16};
    v4u v[3], zz[2];
    const int jj = l & 3, r16 = (l >> 2) ^ ((jj >> 1) << 3);
    const u32 a = opq(V_MEM + (jj * 16 + ((c.wave ^ jj) & 1) * 8 + (r16 & 7)) * 16 + (r16 >> 3) * 8);
    const u32 ra = opq(V_MEM + c.wave * 1024 + (l16 ^ ((l & 16) << 3))), l4 = opq(r16 * 16 + jj * 4);
    __builtin_amdgcn_s_setprio(3);
    __builtin_amdgcn_s_sleep(SLP);
    TS(c, 5);
    zz[0] = gld(c, zoff[0], l16); zz[1] = gld(c, zoff[1], l16);
    __builtin_amdgcn_s_sleep(PSLEEP);
#pragma unroll
    for (int k = 0; k < 3; ++k) v[k] = gld(c, off[k], voff[k]);
    SB();
    const bool zm = __builtin_amdgcn_ballot_w64(umax(max4(zz[0]), max4(zz[1])) == SENT) != 0;
    if (zm) { asm volatile("" ::: "memory"); zz[0] = gld(c, zoff[0], l16); zz[1] = gld(c, zoff[1], l16); }
    SB();
    if (__builtin_amdgcn_ballot_w64(umax(umax(max4(v[0]), max4(v[1])), max4(v[2])) == SENT) != 0) poll<3>(c, off, voff, v);
    if (zm && __builtin_amdgcn_ballot_w64(umax(max4(zz[0]), max4(zz[1])) == SENT) != 0) poll<2>(c, zoff, zvoff, zz);
    TS(c, 6);
    const f4 z = {0, 0, 0, 0};
    f4 lo1 = mfma(u[1][0], v[0], z), hi0 = mfma(u[0][1], v[0], z);
    lo1 = mfma(u[1][2], v[1], lo1); hi0 = mfma(u[0][3], v[1], hi0);
    lo1 = mfma(zz[1], v[2], lo1);
    f4 lo0 = mfma(u[0][0], v[0], z), hi1 = mfma(u[1][1], v[0], z);
    SB();
    f4 r, w;
    lo0 = mfma(u[0][2], v[1], lo0);
#pragma unroll
    for (int i = 0; i < 4; ++i) w[i] = opqf((l & 8) ? hi0[i] : lo1[i]);
    SB();
    hi1 = mfma(u[1][3], v[1], hi1);
    lo0 = mfma(zz[0], v[2], lo0);
    SB();
#pragma unroll
    for (int i = 0; i < 4; ++i) r[i] = opqf(((l & 8) ? hi1[i] : lo0[i]) + ror8(w[i]));
    st128(ra, __builtin_bit_cast(v4u, r));
    bar();
    if (c.wave < 2) {
        f2 q[8];
#pragma unroll
        for (int w = 0; w < 8; ++w) q[w] = lds64f(a + w * 1024);
        const f2 s = ((q[0] + q[1]) + (q[2] + q[3])) + ((q[4] + q[5]) + (q[6] + q[7]));
        gst32(c, mo, l4, pkc(s.x, s.y));
    } else __builtin_amdgcn_s_sleep(BSLEEP);
    __builtin_amdgcn_s_setprio(0);
    TS(c, 7);
}

template <class F> DI void out_part(Ctx& c, int t, int j, f4 (&a)[2], F mid) {
    const u32 l16 = lane_id() * 16;
    a[0] = f4{0, 0, 0, 0}; a[1] = f4{0, 0, 0, 0};
    v4u u[4], px[4][2], z[3];
#pragma unroll
    for (int k = 0; k < 4; ++k) {
        u[k] = gld(c, ub(c, t, j & 3) + (j >> 2) * 65536 + io(c.wave * 4096), l16 + k * 1024);
        px[k][0] = gld(c, pxb(c, t) + c.wave * 16384 + (k >> 1) * 4096, l16 + (k & 1) * 2048);
        px[k][1] = gld(c, pxb(c, t) + c.wave * 16384 + (k >> 1) * 4096, l16 + (k & 1) * 2048 + 1024);
    }
    const u32 po = pxb(c, t) + 131072 + io(c.wave * 2048);
    const u32 off[1] = {zb(c, t) + j * 16384 + c.wave * 1024}; const u32 voff[1] = {l16};
    SB();
    mid();
    SB();
    a[0] = mfma(u[0], px[0][0], a[0]); a[1] = mfma(u[0], px[0][1], a[1]);
    SB();
    z[0] = gld(c, off[0], l16); z[1] = gld(c, po, l16); z[2] = gld(c, po, l16 + 1024);
    SB();
#pragma unroll
    for (int k = 1; k < 4; ++k) { a[0] = mfma(u[k], px[k][0], a[0]); a[1] = mfma(u[k], px[k][1], a[1]); }
    SB();
    if (__builtin_amdgcn_ballot_w64(max4(z[0]) == SENT) != 0) { v4u zz[1]; poll<1>(c, off, voff, zz); z[0] = zz[0]; }
    a[0] = mfma(z[0], z[1], a[0]); a[1] = mfma(z[0], z[2], a[1]);
}
DI void out_store(const Ctx& c, int t, int j, int m, f4 s) {
    const int l = lane_id();
    v2u o = {pk(s[0], s[1]), pk(s[2], s[3])};
    const __amdgpu_buffer_rsrc_t orr = __builtin_amdgcn_make_buffer_rsrc((void*)c.out, 0, 2048 * 2560 - 1280, 0x00020000);
    __builtin_amdgcn_raw_buffer_store_b64(o, orr, (l & 15) * 2560 + (l >> 4) * 8, (t * 32 + m * 16) * 2560 + j * 32, OAUX);
}
template <u32 LR, class F> DI void out_duty(Ctx& c, int t, int j, F mid) {
    f4 a[2];
    out_part(c, t, j, a, [] {});
    const u32 l16 = lane_id() * 16;
    mid();
    if (c.wave != 0) st128(LR + (c.wave - 1) * 1024 + l16, __builtin_bit_cast(v4u, a[0]));
    if (c.wave != 1) st128(LR + 7168 + (c.wave ? c.wave - 1 : 0) * 1024 + l16, __builtin_bit_cast(v4u, a[1]));
    bar();
    if (c.wave < 2) {
        f4 s = c.wave ? a[1] : a[0];
#pragma unroll
        for (int w = 0; w < 7; ++w) s += __builtin_bit_cast(f4, lds128(LR + c.wave * 7168 + w * 1024 + l16));
        out_store(c, t, j, c.wave, s);
    }
    bar();
}

template <bool WO> DI TB wbase(const Ctx& c, int l) {
    return mktb(WO ? 12288 + (l >> 4) * 1024 + (c.wave & 3) * 256 + rowp(l) : (c.wave >> 2) * 12288 + (l >> 4) * 3072 + (c.wave & 3) * 768 + rowp(l));
}
template <bool WO> DI void issue_chunk(const unsigned char* wb, u32 voff, int wave, int ch) {
    const u32 slot = ((ch + 2) % 6) * 24576;
#pragma unroll
    for (int u = 0; u < 3; ++u) {
        if (WO) dma(wb + (size_t)(3 * (ch & 1) + u) * 40960 + (ch >> 1) * 128, voff, slot + 12288 + (u * 4 + (wave & 3)) * 1024);
        else dma(wb + (size_t)ch * 393216 + u * 128, voff, slot + (wave >> 2) * 12288 + (wave & 3) * 3072 + u * 1024);
    }
}
template <bool WO> DI void load_weights(const Ctx& c, const u16* w, v8u (&W)[24]) {
    const int l = lane_id();
    const int kb = c.wave & 3, rr = ((l >> 1) & 7) ^ (4 * (kb & 1));
    const u32 voff = (8 * kb + rr) * (WO ? 1280 : 12288) + (l >> 4) * 32 + (l & 1) * 16;
    const unsigned char* wb = (const unsigned char*)w + (WO ? (size_t)c.h * 245760 : (size_t)c.h * 384);
    const TB wl = wbase<WO>(c, l);
#pragma unroll
    for (int i = 0; i < 5; ++i) issue_chunk<WO>(wb, voff, c.wave, i);
#pragma unroll
    for (int i = 0; i < 16; ++i) {
        asm volatile("s_waitcnt vmcnt(12) lgkmcnt(0)\n\ts_barrier" ::: "memory");
        if (i < 15) issue_chunk<WO>(wb, voff, c.wave, i + 5);
        const u32 slot = ((i + 2) % 6) * 24576;
#pragma unroll
        for (int u = 0; u < 3; ++u) {
            v4u f = trfrag(wl, slot + u * (WO ? 4096 : 256));
            const int ks = 3 * (i & 1) + u;
            const int pi = WO ? (i >> 1) * 3 + (ks >> 1) : u * 8 + (i >> 1), hf = WO ? (ks & 1) : (i & 1);
            W[pi] = hf ? cat8(lo4(W[pi]), f) : cat8(f, f);
        }
    }
    barv();
}

template <u32 LM, class F> DI u32 crit_gemm1(Ctx& c, int s, const v8u (&W)[24], int l, u32 d01, u32 d2, u32 mi, F late) {
    TB wl = wbase<false>(c, l);
    v8u T[3];
#pragma unroll
    for (int u = 0; u < 3; ++u) T[u] = cat8(trfrag(wl, u * 256), trfrag(wl, 24576 + u * 256));
    wl.a = opq(wl.a + 49152); wl.b = opq(wl.b + 49152);
    const u32 idx = opq(0x4444u + (l & 8) * 0x1555u + ((l & 8) >> 2) + (LM == L_MEM ? ((l >> 4) * 256 + rowp(l)) << 16 : 0u)), ab = opq(LM + l * 16);
    d01 = opq(d01); d2 = opq(d2);
    f4 acc[3] = {{0, 0, 0, 0}, {0, 0, 0, 0}, {0, 0, 0, 0}};
    asm volatile("" : "+v"(acc[0]), "+v"(acc[1]), "+v"(acc[2]));
    TS(c, 0);
    stage_mem<LM>(c, s, mi);
    TS(c, 1);
    v4u a[11];
    a[0] = lds128(ab + 8192);
    SB();
#pragma unroll
    for (int i = 1; i < 4; ++i) a[i] = lds128(ab + (i - 1) * 1024);
    SB();
#pragma unroll
    for (int i = 0; i < 10; ++i) {
#pragma unroll
        for (int u = 0; u < 3; ++u) acc[u] = smf(a[i], (i == 0 || i == 9) ? T[u] : W[u * 8 + i - 1], acc[u], idx);
        SB();
        if (i + 4 < 10) a[i + 4] = lds128(ab + (i + 4 == 9 ? 9 : i + 3) * 1024);
        if (i >= 3 && i < 6) T[i - 3] = cat8(trfrag(wl, (i - 3) * 256), trfrag(wl, 24576 + (i - 3) * 256));
        late(i);
        SB();
        if (LM == L_MEM && i == 0) TS2(c, 5);
    }
    if (LM == L_MEM) TS2(c, 6);
    float r[4];
#pragma unroll
    for (int i = 0; i < 4; ++i) r[i] = fold32(acc[0][i], acc[1][i]);
    st64(d01, v2u{pk(r[0], r[1]), pk(r[2], r[3])});
    st32(d2, pk(fold32(acc[2][0], acc[2][2]), fold32(acc[2][1], acc[2][3])));
    if (LM == L_MEM) TS2(c, 7);
    SB();
    return idx >> 16;
}

DI constexpr int step_ks(int q) { return q < 8 ? ((q & 1) ? 16 + (q >> 1) : (q >> 1)) : q - 4; }
DI constexpr u32 step_slot(int q) { return (step_ks(q) >= 16 ? step_ks(q) - 16 : step_ks(q) + 4) * 2048; }
template <bool VM, class F> DI void bulk_gemm1(Ctx& c, const v8u (&W)[24], int s, F hook) {
    hook(-1);
    const int l = lane_id();
    const TB wl = wbase<false>(c, l);
    const u32 ab = opq((VM ? V_X : L_X) + xfrag(l));
    f4 acc[2][3];
#pragma unroll
    for (int m = 0; m < 2; ++m)
#pragma unroll
        for (int u = 0; u < 3; ++u) acc[m][u] = f4{0, 0, 0, 0};
    v4u a[22][2], b[5][3];
#pragma unroll
    for (int i = 0; i < 4; ++i) a[i >> 1][i & 1] = lds128(ab + step_slot(i >> 1) + (i & 1) * 1024);
#pragma unroll
    for (int u = 0; u < 3; ++u) b[0][u] = trfrag(wl, u * 256);
#pragma unroll
    for (int q = 0; q < 20; ++q) {
        const int ks = step_ks(q);
        if (VM && q == 10) {
            const int l2 = lane_id();
            const unsigned char* g = (const unsigned char*)c.x + (size_t)s * 40960 + 960;
            const u32 vo = (l2 & 15) * 1280 + (l2 >> 4) * 16;
            a[19][0] = *(const v4u*)(g + vo); a[19][1] = *(const v4u*)(g + 20480 + vo);
        }
        hook(q);
        SB();
#pragma unroll
        for (int m = 0; m < 2; ++m) {
#pragma unroll
            for (int u = 0; u < 3; ++u) {
                acc[m][u] = mfma(a[q][m], ks >= 16 ? b[ks - 16][u] : ((ks & 1) ? hi4(W[u * 8 + (ks >> 1)]) : lo4(W[u * 8 + (ks >> 1)])), acc[m][u]);
                if (m == 1 && ks >= 16 && ks < 19) { SB(); b[ks - 15][u] = trfrag(wl, (ks - 15) * 24576 + u * 256); SB(); }
            }
            SB();
            if (q < (VM ? 17 : 18)) a[q + 2][m] = lds128(ab + step_slot(q + 2) + m * 1024);
            SB();
        }
    }
    const int l3 = lane_id(), cc = l3 & 15;
    const u32 ia = L_IMG + (c.wave >> 2) * 12288 + ((c.wave & 3) * 48 + (cc ^ (4 * (cc >> 3)))) * 32 + (l3 >> 4) * 8;
    v2u v[2][3];
#pragma unroll
    for (int m = 0; m < 2; ++m)
#pragma unroll
        for (int u = 0; u < 3; ++u) v[m][u] = v2u{pk(acc[m][u][0], acc[m][u][1]), pk(acc[m][u][2], acc[m][u][3])};
    hook(20);
#pragma unroll
    for (int m = 0; m < 2; ++m)
#pragma unroll
        for (int u = 0; u < 3; ++u) st64(ia + m * 6144 + u * 512, v[m][u]);
}

DI bool out_vm(const Ctx& c) { return c.h >= 20 && c.h < 28; }
DI void vm_duty(Ctx& c, int s) {
    if (s + 6 < NSEG && c.wave >= 4 && c.wave < 7) {
        load_x<2, 2>(c, s + 6, 16 + ((c.wave >> 1) & 1), c.wave & 1, V_XH);
        load_x<7, 2>(c, s + 6, (c.wave >> 1) & 1, c.wave & 1, V_XL);
        if (c.wave < 6) { load_x<1, 1>(c, s + 6, 14, c.wave & 1, V_XL); load_x<3, 2>(c, s + 6, 1 + (c.wave & 1) * 6, 1, V_XL); }
        else { load_x<2, 2>(c, s + 6, 17, 1, V_XH); load_x<1, 1>(c, s + 6, 13, 1, V_XL); }
    }
    if (out_vm(c) && s >= 0) {
        flag_wait(c, s, true, 1);
        if (s + 2 >= NSEG) out_duty<V_MEM>(c, s, c.h + 12, [] {});
    }
}

template <bool MEM> DI void attn8(Ctx& c, int s, u32 pa) {
    v4u k[6][2], q[6], m[6];
    constexpr int PF = MEM ? MPF : 3;
    if (!MEM) bar();
    const TB il = mktb(L_IMG + 12288 + pa), fl = mktb(L_F + (pa & ~16u));
#pragma unroll
    for (int ks = 0; ks < (MEM ? MPRE : PF); ++ks) { k[ks][0] = trfrag(il, ks * 1024); k[ks][1] = trfrag(il, 6144 + ks * 1024); }
    f4 t[3] = {{0, 0, 0, 0}, {0, 0, 0, 0}, {0, 0, 0, 0}};
    if (MEM) {
        SB();
        asm volatile("s_waitcnt lgkmcnt(%0)\n\ts_barrier" :: "n"(4 * MPRE) : "memory");
#pragma unroll
        for (int ks = 0; ks < 3; ++ks) {
            q[ks] = trfrag(fl, ks * 1024);
            if (ks >= MPRE && ks < PF) { k[ks][0] = trfrag(il, ks * 1024); k[ks][1] = trfrag(il, 6144 + ks * 1024); }
            SB();
        }
        SB();
    }
    const int l = lane_id(), g = l >> 4, cc = l & 15;
    const u32 qo = L_IMG + 6144 - L_F + (c.wave & 1) * 16;
    const TB ql = TB{fl.a + qo, fl.b + qo};
    const u32 po = MEM ? mpb(c, s) : pxb(c, s) + (c.wave & 1) * 128;
    const u32 o1 = MEM ? po + MP_PX + io((c.h >> 1) * 1024) + (c.h & 1) * 128 : po + io(c.h * 2048 + 1024);
    const u32 o2 = MEM ? po + MP_PM + c.h * 128 : po + 131072 + io((c.h >> 2) * 2048 + 1024) + (c.h & 3) * 256;
    const u32 v1 = opq(g * 256 + (cc & 7) * 16 + (cc & 8)), v2 = opq(cc < 8 ? cc * 16 + (g & 1) * 8 + (g >> 1) * 4 : 0x80000000u);
    asm volatile("" :: "s"(o1), "s"(o2));
    if (MEM) {
        __builtin_amdgcn_s_setprio(0);
        SB();
#pragma unroll
        for (int ks = 0; ks < 6; ++ks) {
            t[1] = mfma(k[ks][0], q[ks], t[1]);
            t[2] = mfma(k[ks][1], q[ks], t[2]);
            SB();
            if (ks + PF < 6) { k[ks + PF][0] = trfrag(il, (ks + PF) * 1024); k[ks + PF][1] = trfrag(il, 6144 + (ks + PF) * 1024); }
            if (ks < 3) q[ks + 3] = trfrag(fl, (ks + 3) * 1024);
            SB();
        }
        __builtin_amdgcn_s_setprio(3);
        bar();
        TS(c, 2);
#pragma unroll
        for (int ks = 0; ks < 6; ++ks) { m[ks] = trfrag(fl, ks * 1024 + 16); SB(); }
    }
    float v[5], mx;
    if (!MEM) {
        bar();
        __builtin_amdgcn_s_sleep(ASLEEP);
        TS(c, 2);
        q[0] = trfrag(ql, 0); q[1] = trfrag(ql, 1024); m[0] = trfrag(fl, 16);
        SB();
#pragma unroll
        for (int ks = 0; ks < 6; ++ks) {
            t[1] = mfma(k[ks][0], q[ks], t[1]);
            SB();
            if (ks < 5) m[ks + 1] = trfrag(fl, (ks + 1) * 1024 + 16);
            SB();
            t[2] = mfma(k[ks][1], q[ks], t[2]);
            SB();
            if (ks + PF < 6) { k[ks + PF][0] = trfrag(il, (ks + PF) * 1024); k[ks + PF][1] = trfrag(il, 6144 + (ks + PF) * 1024); }
            SB();
            t[0] = mfma(m[ks], q[ks], t[0]);
            SB();
            if (ks < 4) q[ks + 2] = trfrag(ql, (ks + 2) * 1024);
            SB();
        }
    }
    const float sc = 0.07216878364870322f * 1.4426950408889634f;
#pragma unroll
    for (int i = 0; i < 4; ++i) {
        v[i] = cc < 8 ? t[1][i] : t[2][i];
        if (!MEM) v[i] = (4 * g + i + 8 > (c.wave & 1) * 8 + cc && cc >= 8) ? __uint_as_float(opq(0xFF800000u)) : v[i];
    }
    mx = opqf(fmx(fmx(v[0], v[1]), fmx(v[2], v[3])));
    if (MEM) {
        SB();
#pragma unroll
        for (int ks = 0; ks < 6; ++ks) t[0] = mfma(m[ks], q[ks], t[0]);
        SB();
    }
    v[4] = g < 2 ? (cc < 8 ? t[0][0] : t[0][1]) : (cc < 8 ? t[0][2] : t[0][3]);
    mx = fmx(mx, v[4]);
    mx = xmax(fmx(mx, ror8(mx))) * sc;
#pragma unroll
    for (int i = 0; i < 5; ++i) v[i] = __builtin_amdgcn_exp2f(__builtin_fmaf(v[i], sc, -mx));
    float sum = (v[0] + v[1]) + (v[2] + v[3]) + v[4];
    sum = xsum(sum + ror8(sum));
    const float inv = __builtin_amdgcn_rcpf(sum);
    gst64(c, o1, v1, v2u{pkc(v[0] * inv, v[1] * inv), pkc(v[2] * inv, v[3] * inv)});
    const float w = v[4] * inv;
    gst32(c, o2, v2, pkc(w, ror8(w)));
    __builtin_amdgcn_s_setprio(0);
}

DI void attn16(Ctx& c, int s) {
    bar();
    const int l = lane_id(), g = l >> 4, cc = l & 15;
    const TB il = mktb(L_IMG + g * 256 + rowp(l));
    f4 t[2] = {{0, 0, 0, 0}, {0, 0, 0, 0}};
    v4u q[6], k[6], m[6];
#pragma unroll
    for (int ks = 0; ks < 4; ++ks) { q[ks] = trfrag(il, ks * 1024); k[ks] = trfrag(il, 12288 + ks * 1024); }
    bar();
    __builtin_amdgcn_s_sleep(ASLEEP);
    TS(c, 2);
    m[0] = trfrag(il, L_F - L_IMG); m[1] = trfrag(il, L_F - L_IMG + 1024);
    SB();
#pragma unroll
    for (int ks = 0; ks < 6; ++ks) {
        t[1] = mfma(k[ks], q[ks], t[1]); t[0] = mfma(m[ks], q[ks], t[0]);
        SB();
        if (ks < 4) m[ks + 2] = trfrag(il, L_F - L_IMG + (ks + 2) * 1024);
        if (ks < 2) { q[ks + 4] = trfrag(il, (ks + 4) * 1024); k[ks + 4] = trfrag(il, 12288 + (ks + 4) * 1024); }
        SB();
    }
    const float sc = 0.07216878364870322f * 1.4426950408889634f, ninf = __uint_as_float(opq(0xFF800000u));
    float mx = ninf;
#pragma unroll
    for (int k = 0; k < 2; ++k)
#pragma unroll
        for (int i = 0; i < 4; ++i) {
            const bool vis = k == 0 ? g >= 2 : (4 * g + i <= cc);
            t[k][i] = vis ? t[k][i] : ninf; mx = fmx(mx, t[k][i]);
        }
    mx = xmax(mx) * sc;
    float sum = 0;
#pragma unroll
    for (int k = 0; k < 2; ++k)
#pragma unroll
        for (int i = 0; i < 4; ++i) { t[k][i] = __builtin_amdgcn_exp2f(__builtin_fmaf(t[k][i], sc, -mx)); sum += t[k][i]; }
    sum = xsum(sum);
    const float inv = __builtin_amdgcn_rcpf(sum);
    const v4u px = {pk(t[1][0] * inv, t[1][1] * inv), pk(t[1][2] * inv, t[1][3] * inv), 0u, 0u};
    const v2u pm = {pk(t[0][0] * inv, t[0][1] * inv), pk(t[0][2] * inv, t[0][3] * inv)};
    const u32 po = pxb(c, s);
    gst(c, po + io(c.h * 2048), l * 16, px);
    if (g >= 2) gst64(c, po + 131072 + io((c.h >> 2) * 2048) + (c.h & 3) * 256, cc * 16 + (g & 1) * 8, pm);
    __builtin_amdgcn_s_setprio(0);
}
DI void load_x_main(const Ctx& c, int s) {
    const int pw = (c.wave >> 1) ^ 2;
    load_x<1, 1>(c, s, 16 + pw, c.wave & 1, L_XH);
    load_x<2, 1>(c, s, 2 * pw, c.wave & 1, L_XL);
}

DI void qk_main(Ctx& c, const u16* wq, const u16* wk) {
    v8u W[24];
    if (c.par == 0 && c.p == 1 && c.h < NPAGE && c.wave == 0) probe_prep(c, c.h);
    load_x<2, 4>(c, c.p, 8 + (c.wave >> 1), c.wave & 1, L_XL);
    load_weights<false>(c, c.wave < 4 ? wq : wk, W);
    if (c.par == 0 && c.p == 1 && c.h < NPAGE) { if (c.wave == 0) probe_page(c, c.h); bar(); }
    load_x_main(c, c.p);
    barv();
    for (int s = c.p - 2; s < NSEG; s += 2) {
        asm volatile("" : "+s"(c.wave), "+s"(c.h), "+s"(c.par));
        if (s >= 0) {
            const int l = lane_id(), g = l >> 4, cc = l & 15;
            const u32 fa = L_F + ((c.wave & 3) * 48 + (cc ^ (4 * (cc >> 3)))) * 32 + (c.wave >> 2) * 16 + (g & 1) * 8;
            if (c.wave == 0) __builtin_amdgcn_s_setprio(3);
            else if (c.wave < 4) __builtin_amdgcn_s_setprio(2);
            else __builtin_amdgcn_s_setprio(1);
            const u32 pa = crit_gemm1<L_MEM>(c, s, W, l, fa + (g >> 1) * 512, fa + 1024 + (g >> 1) * 4, mem_in(c, s), [&](int i) {
                if (i == QB) asm volatile("s_cmp_lt_u32 %0, 4\n\ts_cbranch_scc1 1f\n\ts_barrier\n1:" :: "s"(c.wave) : "memory", "scc");
            });
            if (c.wave == 0) attn8<true>(c, s, pa);
            else if (c.wave == 1) attn16(c, s);
            else if (c.wave < 4) attn8<false>(c, s, pa);
            else {
                bar();
                __builtin_amdgcn_s_setprio(0);
                __builtin_amdgcn_s_sleep(KSLEEP);
                TS(c, 2);
                if (c.wave >= 6 && s + 2 < NSEG) load_x<5, 1>(c, s + 2, 8, c.wave & 1, L_XL);
                if (c.wave == 4) __builtin_amdgcn_s_sleep(QSLEEP);
            }
            TS(c, 3);
            if (s < 2) {
                const v4u sv = sentv();
                for (int i = c.wave * 64 + lane_id(); i < 30 * 40; i += 512) {
                    const int st = c.p + 4 + 2 * (i / 40), e = i % 40;
                    const u32 o = e < 32 ? MP_PX + io((c.h >> 1) * 1024) + ((e >> 3) * 16 + (c.h & 1) * 8 + (e & 7)) * 16 : MP_PM + c.h * 128 + (e & 7) * 16;
                    gst(c, 0, mpb(c, st) + o, sv);
                }
            }
        }
        auto flag_p = [&] { gst32(c, fb(c, s < 0 ? 0 : s) + 512 + c.h * 4, c.wave == 7 && s >= 0 ? 0u : 0x80000000u, opq(1u)); };
        if (s + 2 < NSEG) {
            f4 a[2];
            if (c.wave < 4) {
                bar();
                TS1(c, 5);
                if (c.wave < 2 && s >= 0) load_x<3, 1>(c, s + 2, 13, c.wave & 1, L_XL);
                if (s >= 2) {
                    out_part(c, s - 2, c.h, a, [] { asm volatile("s_waitcnt vmcnt(12)\n\ts_barrier" ::: "memory"); });
                    const u32 pa = L_IMG + c.wave * 2048 + lane_id() * 16;
                    st128(pa, __builtin_bit_cast(v4u, a[0])); st128(pa + 1024, __builtin_bit_cast(v4u, a[1]));
                } else barv();
                TS1(c, 6);
            }
            bulk_gemm1<false>(c, W, s + 2, [&](int q) {
                if (q == QM1) bar_half<true, true>(c.wave);
                if (q == QM0) bar_half<false, true>(c.wave);
                if (q == 15) bar_half<false, false>(c.wave);
                if (q == 20) { TS1(c, 7); bar_half<true, false>(c.wave); }
            });
            if (c.wave >= 4) {
                TS1(c, 5);
                if (s >= 2) {
                    out_part(c, s - 2, c.h, a, [&] { asm volatile("s_barrier" ::: "memory"); flag_p(); });
                    const u32 l16 = lane_id() * 16, pa = L_IMG + (c.wave - 4) * 2048 + l16;
                    a[0] += __builtin_bit_cast(f4, lds128(pa)); a[1] += __builtin_bit_cast(f4, lds128(pa + 1024));
                    const u32 pb = L_X + (c.wave >= 6 ? c.wave - 2 : 0) * 2048 + l16;
                    if (c.wave != 4) st128(pb, __builtin_bit_cast(v4u, a[0]));
                    if (c.wave != 5) st128(pb + 1024, __builtin_bit_cast(v4u, a[1]));
                    TS1(c, 6);
                    bar();
                    if (c.wave < 6) {
                        const u32 pc = L_X + (c.wave & 1) * 1024 + l16;
                        f4 r = (c.wave & 1) ? a[1] : a[0];
                        r += __builtin_bit_cast(f4, lds128(pc));
                        r += __builtin_bit_cast(f4, lds128(pc + 8192)) + __builtin_bit_cast(f4, lds128(pc + 10240));
                        out_store(c, s - 2, c.h, c.wave & 1, r);
                    }
                } else { bar(); flag_p(); bar(); }
            }
            TS(c, 4);
            if (s + 4 < NSEG) load_x_main(c, s + 4);
            if (s < 0) get_mask(c);
            else flag_wait(c, s, false, 1);
        } else {
            barv(); flag_p();
            for (int t = s - 2; t <= s; t += 2) { flag_wait(c, t, false, 1); out_duty<L_MEM>(c, t, c.h, [] {}); }
        }
        TSF(c, s);
    }
}

DI u32 wo_off(int i, int ks) { return ((i - 8) * 2 + ks / 3) * 24576 + (ks % 3) * 4096; }
DI u32 z_lane(int l) { const int g = l >> 4; return ((g >> 1) + 2 * (g & 1)) * 65536 + (l & 15) * 16; }
DI void swap32(f4& a, f4& b, int i) {
    auto s = __builtin_amdgcn_permlane32_swap(__float_as_uint(a[i]), __float_as_uint(b[i]), false, false);
    a[i] = __uint_as_float(s[0]); b[i] = __uint_as_float(s[1]);
}
DI v4u z_pack(const u32 (&x)[2], const u32 (&y)[2]) {
    auto s0 = __builtin_amdgcn_permlane16_swap(x[0], y[0], false, false);
    auto s1 = __builtin_amdgcn_permlane16_swap(x[1], y[1], false, false);
    return v4u{s0[0], s1[0], s0[1], s1[1]};
}
template <class F> DI void z_tail(Ctx& c, u32 zo, TB wl, TB wh, TB il, F mid) {
    v4u a[3]; v8u T[2][3];
    asm volatile("" ::: "memory");
    SB();
    T[0][0] = cat8(trfrag(wl, wo_off(8, 0)), trfrag(wl, wo_off(8, 1))); T[1][0] = cat8(trfrag(wh, wo_off(8, 0)), trfrag(wh, wo_off(8, 1)));
    asm volatile("s_waitcnt lgkmcnt(8)\n\ts_barrier" ::: "memory");
    TS(c, 2);
#pragma unroll
    for (int f = 0; f < 3; ++f) a[f] = trfrag(il, f * 1024);
    SB();
    const int l = lane_id(), g = l >> 4;
    const u32 idx = 0x4444u + (l & 8) * 0x1555u + ((l & 8) >> 2);
    f4 acc[2] = {{0, 0, 0, 0}, {0, 0, 0, 0}};
    mid();
    SB();
#pragma unroll
    for (int f = 0; f < 3; ++f)
#pragma unroll
        for (int j = 0; j < 2; ++j) {
            acc[j] = smf(a[f], T[j][f], acc[j], idx);
            SB();
            if (f < 2) T[j][f + 1] = cat8(trfrag(j ? wh : wl, wo_off(8, 2 * f + 2)), trfrag(j ? wh : wl, wo_off(8, 2 * f + 3)));
            SB();
        }
    const u32 x[2] = {pkc(fold32(acc[0][0], acc[1][0]), fold32(acc[0][1], acc[1][1])), pkc(fold32(acc[0][2], acc[1][2]), fold32(acc[0][3], acc[1][3]))};
    gst(c, zo, (g & 1) ? 0x80000000u : z_lane(l), z_pack(x, x));
    __builtin_amdgcn_s_setprio(0);
}

DI void v_main(Ctx& c, const u16* wv) {
    v8u W[24];
    load_weights<false>(c, wv, W);
    NodeA na = {};
    for (int s = c.p - 4; s < NSEG; s += 2) {
        asm volatile("" : "+s"(c.wave), "+s"(c.h), "+s"(c.par));
        if (s >= 0) {
            const int l = lane_id(), g = l >> 4, cc = l & 15;
            const int rho = ((cc >> 2) << 1) | (cc & 1);
            const u32 a = V_VS + ((cc >> 1) & 1) * 16 + (g & 1) * 8;
            const int t0 = c.wave * 3 + (g >> 1), t2 = c.wave * 3 + 2;
            TB wl, wh, il;
            const u32 pa = opq((12288 + g * 1024 + c.wave * 256 + rowp(l)) | (g * 256 + rowp(l)) << 16);
            __builtin_amdgcn_s_setprio(VPRIO);
            crit_gemm1<V_MEM>(c, s, W, l, a + (t0 * 8 + (rho ^ (4 * (t0 & 1)))) * 32, a + (t2 * 8 + (rho ^ (4 * (t2 & 1)))) * 32 + (g >> 1) * 4,
                na.mi, [&](int i) {
                    if (i == 7) { wl = mktb(pa & 0xFFFFu); il.a = opq(V_VS + (pa >> 16)); }
                    if (i == 8) { il.b = opq(il.a ^ 128u); wh = TB{opq(wl.a + 49152), opq(wl.b + 49152)}; }
                });
            v4u u[2][4];
            z_tail(c, na.zo + 2 * 262144, wl, wh, il, [&] { node_u<0, VPRE>(c, na, u); });
            TS(c, 6);
            if (out_vm(c) && s >= 2) {
                f4 a[2];
                __builtin_amdgcn_s_sleep(OVSLEEP);
                out_part(c, s - 2, c.h + 12, a, [] {});
                const u32 pa = V_MEM + c.wave * 2048 + lane_id() * 16;
                st128(pa, __builtin_bit_cast(v4u, a[0])); st128(pa + 1024, __builtin_bit_cast(v4u, a[1]));
                TS(c, 5);
            } else if (c.h >= 20) { if (c.h >= 28 && s + 8 < NSEG) { prefetch_x(c, s + 8, V_DUMP); prefetch_x(c, s + 8, V_DUMP, 4); } }
            else if (s < NSEG - 1) { node_u<VPRE, 8>(c, na, u); node_duty<NSLEEP + NVX>(c, na, u); }
            if (c.h < 20 && s < 2) {
                const v4u sv = sentv();
                for (int i = c.wave * 64 + lane_id(); i < 30 * 32; i += 256) {
                    const int st = 5 + c.p + 2 * (i >> 5);
                    if (st < NSEG) gst(c, 0, mpb(c, st) + io(c.h * 512) + (i & 31) * 16, sv);
                }
            }
        } else {
            if (s >= -2) get_mask(c);
            TS(c, 6);
            barv();
            TS(c, 5);
        }
        if (s + 4 < NSEG) bulk_gemm1<true>(c, W, s + 4, [&](int q) {
            if (q == 9) na = node_addr(c, (s + 2) & 63);
            if (q == 20) { TS(c, 3); bar(); }
        });
        else { na = node_addr(c, (s + 2) & 63); bar(); }
        if (out_vm(c) && s >= 2) bar();
        TS(c, 4);
        vm_duty(c, s);
        TSF(c, s);
    }
}

DI void bulk_gemm2(const Ctx& c, const v8u (&W)[24], int s) {
    const int l = lane_id();
    const TB wl = wbase<true>(c, l), il = mktb(L_IMG + (l >> 4) * 256 + rowp(l));
    const u32 uo = ub(c, s, c.wave & 3) + io(c.h * 1024);
    v4u a[20][2], b[20];
#pragma unroll
    for (int i = 0; i < 4; ++i) a[i >> 1][i & 1] = trfrag(il, (i & 1) * 6144 + (i >> 1) * 1024);
#pragma unroll
    for (int ig = 0; ig < 3; ++ig) {
        const int nt = ig == 0 ? 4 : 3;
        f4 acc[2][4];
#pragma unroll
        for (int j = 0; j < 4; ++j) { acc[0][j] = f4{0, 0, 0, 0}; acc[1][j] = f4{0, 0, 0, 0}; }
#pragma unroll
        for (int ks = 0; ks < 6; ++ks) {
            const int st = ig * 6 + ks;
#pragma unroll
            for (int m = 0; m < 2; ++m) {
                SB();
#pragma unroll
                for (int j = 0; j < nt; ++j) {
                    const int i = ig == 0 ? j : (j < 2 ? 2 + 2 * ig + j : 7 + ig);
                    acc[m][j] = mfma(a[st][m], i < 8 ? ((ks & 1) ? hi4(W[i * 3 + (ks >> 1)]) : lo4(W[i * 3 + (ks >> 1)])) : b[st], acc[m][j]);
                    if (m == 0 && j == 0 && st >= 5 && st < 17) { SB(); b[st + 1] = trfrag(wl, wo_off(7 + (st + 1) / 6, (st + 1) % 6)); SB(); }
                }
                SB();
                if (st < 16) a[st + 2][m] = trfrag(il, m * 6144 + ((st + 2) % 6) * 1024);
            }
        }
        SB();
#pragma unroll
        for (int j = 0; j < nt; ++j) {
            const int i = ig == 0 ? j : (j < 2 ? 2 + 2 * ig + j : 7 + ig);
            const v4u v = {pk(acc[0][j][0], acc[0][j][1]), pk(acc[0][j][2], acc[0][j][3]), pk(acc[1][j][0], acc[1][j][1]), pk(acc[1][j][2], acc[1][j][3])};
            gst(c, uo + i * 65536, l * 16, v);
        }
        SB();
    }
}

DI void o_main(Ctx& c, const u16* wo) {
    v8u W[24];
    load_weights<true>(c, wo, W);
    load_x<2, 2>(c, c.p, 16 + ((c.wave >> 1) & 1), c.wave & 1, V_XH);
    load_x<4, 2>(c, c.p, (c.wave >> 1) & 1, c.wave & 1, V_XL);
    for (int s = c.p - 4; s < NSEG; s += 2) {
        asm volatile("" : "+s"(c.wave), "+s"(c.h), "+s"(c.par));
        if (s >= 0) {
            const int l = lane_id(), g = l >> 4;
            const TB il = mktb(V_VS + g * 256 + rowp(l));
            const u32 idx = opq(0x4444u + (l & 8) * 0x1555u + ((l & 8) >> 2)), za = opq(z_lane(l));
            const NodeA na = node_addr(c, s);
            u32 zo = na.zo, zo1 = zo + 262144;
            asm volatile("" : "+s"(zo1));
            stage_mem<V_MEM>(c, s, na.mi);
            v4u u[2][4];
            if (c.h < 20) flag_wait(c, s, true, 1);
            node_u<0, UPRE>(c, na, u);
            f4 acc[8];
#pragma unroll
            for (int i = 0; i < 4; ++i) { acc[i] = f4{0, 0, 0, 0}; asm volatile("" : "+v"(acc[i])); }
            __builtin_amdgcn_s_setprio(OPRIO);
            TS2(c, 6);
            asm volatile("s_waitcnt vmcnt(%0) lgkmcnt(0)\n\ts_barrier" :: "n"(UPRE) : "memory");
            TS(c, 2);
            v4u a[3];
#pragma unroll
            for (int f = 0; f < 3; ++f) a[f] = trfrag(il, f * 1024);
            SB();
            u32 xp[4][2];
#pragma unroll
            for (int pr = 0; pr < 5; ++pr) {
                float x[4];
#pragma unroll
                for (int n = 0; n < 6; ++n) {
                    if (pr < 4) acc[2 * pr + (n & 1)] = smf(a[n >> 1], W[(2 * pr + (n & 1)) * 3 + (n >> 1)], acc[2 * pr + (n & 1)], idx);
                    SB();
                    if (pr > 0) {
                        f4& p = acc[2 * pr - 2]; f4& q = acc[2 * pr - 1];
                        if (n == 1) { swap32(p, q, 0); swap32(p, q, 1); }
                        if (n == 2) { swap32(p, q, 2); swap32(p, q, 3); x[0] = opqf(p[0] + q[0]); }
                        if (n == 3) { x[1] = opqf(p[1] + q[1]); x[2] = opqf(p[2] + q[2]); x[3] = opqf(p[3] + q[3]); }
                        if (n == 4) { xp[pr - 1][0] = pkc(x[0], x[1]); xp[pr - 1][1] = pkc(x[2], x[3]); }
                        if (n == 5 && !(pr & 1)) gst(c, pr == 2 ? zo : zo1, za, z_pack(xp[pr - 2], xp[pr - 1]));
                        if (n >= 4 && pr < 3) { f4& z = acc[2 * pr + n - 2]; z = f4{0, 0, 0, 0}; asm volatile("" : "+v"(z)); }
                    }
                    SB();
                }
            }
            __builtin_amdgcn_s_setprio(0);
            TS(c, 3);
            if (c.h < 20 && s < NSEG - 1) { node_u<UPRE, 8>(c, na, u); node_duty<NSLEEP>(c, na, u); }
        } else {
            if (s >= -2) get_mask(c);
            TS(c, 6);
            barv();
            TS(c, 5);
        }
        const bool bulk = s >= -2 && s + 2 < NSEG;
        if (bulk) {
            __builtin_amdgcn_s_setprio(UPRIO);
            bulk_gemm2(c, W, s + 2);
            __builtin_amdgcn_s_setprio(0);
            const int l = lane_id(), g = l >> 4;
            const v4u sv = sentv();
            const u32 za = z_lane(l);
#pragma unroll
            for (int k = 0; k < 3; ++k)
                if (k < 2 || !(g & 1)) gst(c, zb(c, s + 2) + k * 262144 + (c.wave & 3) * 16384 + c.h * 256, za, sv);
        }
        auto flag_u = [&] { if (bulk) { asm volatile("s_waitcnt vmcnt(0)" ::: "memory"); gst32(c, fb(c, s + 2) + (c.wave & 3) * 128 + c.h * 4, 0, opq(1u)); } };
        if (out_vm(c) && s >= 2) {
            f4 a[2];
            TS(c, 5);
            out_part(c, s - 2, c.h + 12, a, [] {});
            flag_u();
            TS(c, 0);
            bar();
            TS(c, 1);
            const u32 l16 = lane_id() * 16, pa = V_MEM + (c.wave - 4) * 2048 + l16;
            a[0] += __builtin_bit_cast(f4, lds128(pa)); a[1] += __builtin_bit_cast(f4, lds128(pa + 1024));
            const u32 pb = V_X + 4096 + (c.wave >= 6 ? c.wave - 5 : 0) * 8192 + l16;
            if (c.wave != 4) st128(pb, __builtin_bit_cast(v4u, a[0]));
            if (c.wave != 5) st128(pb + 1024, __builtin_bit_cast(v4u, a[1]));
            bar();
            if (c.wave < 6) {
                const u32 pc = V_X + 4096 + (c.wave & 1) * 1024 + l16;
                f4 r = (c.wave & 1) ? a[1] : a[0];
                r += __builtin_bit_cast(f4, lds128(pc));
                r += __builtin_bit_cast(f4, lds128(pc + 8192)) + __builtin_bit_cast(f4, lds128(pc + 16384));
                out_store(c, s - 2, c.h + 12, c.wave & 1, r);
            }
        } else {
            TS(c, 0);
            bar();
            TS(c, 1);
            flag_u();
        }
        TS(c, 4);
        vm_duty(c, s);
        TSF(c, s);
    }
}

__global__ void __launch_bounds__(512, 2) memtr_kernel(Args a) {
    Ctx c;
    const int wg = blockIdx.x;
    c.wave = __builtin_amdgcn_readfirstlane(threadIdx.x >> 6);
    const int br = (wg & 7) >> 2;
    const int idx = (wg >> 3) * 4 + (wg & 3);
    c.h = idx & 31; c.p = idx >> 6;
    c.r = __builtin_amdgcn_make_buffer_rsrc(a.ws, 0, WS_SPAN, 0x00020000);
    c.par = br ? ~0u : 0u;
    c.spin = 0;
    c.x = br ? a.x[1] : a.x[0]; c.mem0 = br ? a.mem0[1] : a.mem0[0]; c.out = a.out + br * 640;
    if (((idx >> 5) & 1) == 0) qk_main(c, br ? a.w[1][0] : a.w[0][0], br ? a.w[1][1] : a.w[0][1]);
    else {
        if (c.h >= 16) prefetch_x(c, c.p + ((c.h >> 2) & 3) * 2, V_XL + 7 * 2048);
        else if (c.h >= 12 && c.wave < 5) {
            const unsigned char* m = (const unsigned char*)c.mem0 + c.wave * 2048;
            dmai<0, 0>(m, lane_id() * 16, V_XL + 7 * 2048); dmai<1024, 0>(m, lane_id() * 16, V_XL + 7 * 2048);
        }
        if (c.wave < 7) { load_x<1, 1>(c, c.p, 8 + c.wave, 0, V_XL); load_x<1, 1>(c, c.p, 8 + c.wave, 1, V_XL); }
        if (c.wave < 4) v_main(c, br ? a.w[1][2] : a.w[0][2]);
        else o_main(c, br ? a.w[1][3] : a.w[0][3]);
    }
}

extern "C" void kernel_launch(void* const* d_in, const int* in_sizes, int n_in, void* d_out, int out_size, void* d_ws, size_t ws_size, hipStream_t stream) {
    Args a;
    for (int b = 0; b < 2; ++b) {
        a.x[b] = (const u16*)d_in[7 * b]; a.mem0[b] = (const u16*)d_in[7 * b + 1];
        for (int k = 0; k < 4; ++k) a.w[b][k] = (const u16*)d_in[7 * b + 2 + k];
    }
    a.out = (u16*)d_out; a.ws = (unsigned char*)d_ws;
    (void)hipMemsetAsync((unsigned char*)d_ws + INIT_OFF, 0xFF, INIT_BYTES, stream);
    memtr_kernel<<<256, 512, 0, stream>>>(a);
}
```
